```python
import torch

def _softmax(x, axis=-1):
    return torch.softmax(x, dim=axis)

def _gelu(x):
    return torch.nn.functional.gelu(x, approximate='tanh')

def _silu(x):
    return torch.nn.functional.silu(x)

def _sigmoid(x):
    return torch.sigmoid(x)

def _layernorm(x, axis=-1, eps=1e-05):
    m = x.mean(dim=axis, keepdim=True)
    v = x.var(dim=axis, keepdim=True, unbiased=False)
    return (x - m) / torch.sqrt(v + eps)

def _scatter_add(tgt, idx, src, axis=0):
    return tgt.clone().index_add_(axis, idx, src)

def _segment_sum(src, seg, n):
    out = torch.zeros((n,) + src.shape[1:], dtype=src.dtype)
    return out.index_add_(0, seg, src)

def _pad(x, pw):
    flat = tuple((v for pair in reversed(pw) for v in pair))
    return torch.nn.functional.pad(x, flat)
kernel_name = 'standalone__molecular_angular_painn__graph'

def setup_inputs():

    _w = lambda a: (lambda t: t.to(torch.bfloat16) if t.is_floating_point() else t)(torch.as_tensor(a))
    N, deg, D = (25000, 16, 128)
    n_rbf = max(16, 4)
    E = N * deg
    cutoff = 5.0
    freqs = (torch.arange(1, n_rbf + 1) * torch.pi / cutoff).to(float)
    return {'sv': _w((lambda *s: torch.randn(*s, dtype=torch.bfloat16) * 0.4)(N, 4, D) * 0.3), 'pos': _w((lambda *s: torch.randn(*s, dtype=torch.bfloat16) * 3.2)(N, 3) * 1.2), 'src': _w((lambda lo, hi, *s: torch.randint(lo, hi, s))(0, N, E).to(torch.int64)), 'dst': _w((lambda lo, hi, *s: torch.randint(lo, hi, s))(0, N, E).to(torch.int64)), 'freqs': _w(freqs), 'w_phi1': _w((lambda *s: torch.randn(*s, dtype=torch.bfloat16) * 3.2)(D, D) / D ** 0.5), 'w_phi2': _w((lambda *s: torch.randn(*s, dtype=torch.bfloat16) * 3.2)(D, 3 * D) / D ** 0.5), 'w_rbf': _w((lambda *s: torch.randn(*s, dtype=torch.bfloat16) * 3.2)(n_rbf, 3 * D) / n_rbf ** 0.5)}

def reference(sv, pos, src, dst, freqs, w_phi1, w_phi2, w_rbf):
    N, _, D = sv.shape
    E = src.shape[0]
    PI, cutoff = (3.141592653589793, 5.0)
    s, v = (sv[:, 0, :].clone(), sv[:, 1:, :].clone())
    r_vec = pos[src] - pos[dst]
    d = torch.sqrt((r_vec * r_vec).sum(-1) + 1e-08)
    r_hat = r_vec / d[:, None]
    rbf = torch.sin(d[:, None] * freqs[None, :]) / d[:, None]
    fcut = 0.5 * (torch.cos(PI * d / cutoff) + 1.0)
    fcut = torch.where(d < cutoff, fcut, 0.0 * d)
    Wf = rbf @ w_rbf * fcut[:, None]
    phi = (_silu(s @ w_phi1) @ w_phi2)[src]
    m = phi * Wf
    m_s, m_vv, m_vr = (m[:, :D].clone(), m[:, D:2 * D].clone(), m[:, 2 * D:].clone())
    ds = _segment_sum(m_s, dst, N)
    dv_e = m_vv[:, None, :] * v[src] + m_vr[:, None, :] * r_hat[:, :, None]
    dv = _segment_sum(dv_e.reshape(E, 3 * D), dst, N).reshape(N, 3, D)
    return sv + torch.concatenate([ds[:, None, :], dv], axis=1)
```

```cpp
#include <hip/hip_runtime.h>
#include <hip/hip_bf16.h>
#include <stdint.h>


typedef __attribute__((ext_vector_type(8))) short bf16x8;
typedef __attribute__((ext_vector_type(16))) float f32x16;
typedef __attribute__((ext_vector_type(4))) unsigned u32x4;
typedef __attribute__((ext_vector_type(2))) __bf16 bf16x2_t;
typedef __attribute__((ext_vector_type(2))) float f32x2_t;
typedef unsigned short u16;

#define DI __device__ __forceinline__

DI float bf2f(u16 x) { return __uint_as_float(((unsigned)x) << 16); }
DI unsigned pack2(float a, float b) {
    f32x2_t v = {a, b};
    bf16x2_t r = __builtin_convertvector(v, bf16x2_t);
    return __builtin_bit_cast(unsigned, r);
}
DI float rb(float a) {
    return __uint_as_float(pack2(a, 0.f) << 16);
}
DI float lo16(unsigned u) { return __uint_as_float(u << 16); }
DI float hi16(unsigned u) { return __uint_as_float(u & 0xffff0000u); }

struct EdgeGeom {
    unsigned rbf[8];
    float fcut, rx, ry, rz;
};

DI bool edge_dist(const u16* __restrict__ pos, int s, int t, float& rx, float& ry, float& rz, float& d) {
    float sx = bf2f(pos[3 * s]), sy = bf2f(pos[3 * s + 1]), sz = bf2f(pos[3 * s + 2]);
    float tx = bf2f(pos[3 * t]), ty = bf2f(pos[3 * t + 1]), tz = bf2f(pos[3 * t + 2]);
    rx = rb(sx - tx); ry = rb(sy - ty); rz = rb(sz - tz);
    float q = rb(rb(rx * rx) + rb(ry * ry) + rb(rz * rz));
    d = rb(sqrtf(rb(q + 1e-08f)));
    return d < 5.0f;
}
DI void edge_rest(const u16* __restrict__ freqs, float rx, float ry, float rz, float d, EdgeGeom& g) {
    g.rx = rb(rx / d); g.ry = rb(ry / d); g.rz = rb(rz / d);
    float c = rb(cosf(rb(rb(3.141592653589793f * d) / 5.0f)));
    g.fcut = rb(0.5f * rb(c + 1.0f));
#pragma unroll
    for (int kp = 0; kp < 8; ++kp) {
        float f0 = bf2f(freqs[2 * kp]), f1 = bf2f(freqs[2 * kp + 1]);
        float a0 = rb(rb(sinf(rb(d * f0))) / d);
        float a1 = rb(rb(sinf(rb(d * f1))) / d);
        g.rbf[kp] = pack2(a0, a1);
    }
}

__global__ __launch_bounds__(256) void k_prep(const u16* __restrict__ w1, const u16* __restrict__ w2,
                                              const u16* __restrict__ wrbf, int* __restrict__ cnt,
                                              u16* __restrict__ w1f, u16* __restrict__ w2f,
                                              unsigned* __restrict__ wr, int N) {
    const int tid = blockIdx.x * 256 + threadIdx.x;
    const int nt = gridDim.x * 256;
    for (int i = tid; i < N; i += nt) cnt[i] = 0;
    for (int i = tid; i < 128 * 128; i += nt) {
        int j = i & 7, lane = (i >> 3) & 63, f = i >> 9;
        int t = f >> 3, ks = f & 7, r = lane & 31, h = lane >> 5;
        int k = 16 * ks + 8 * h + j, hid = 32 * t + r;
        w1f[i] = w1[k * 128 + hid];
    }
    for (int i = tid; i < 128 * 384; i += nt) {
        int j = i & 7, lane = (i >> 3) & 63, f = i >> 9;
        int c = f >> 3, ks = f & 7, r = lane & 31, h = lane >> 5;
        int t = ks >> 1, s = ks & 1;
        int hid = 32 * t + 16 * s + 8 * (j >> 2) + 4 * h + (j & 3), ch = 32 * c + r;
        w2f[i] = w2[hid * 384 + ch];
    }
    for (int i = tid; i < 48 * 64; i += nt) {
        int lane = i & 63, j = i >> 6;
        int kp = j / 6, q = j % 6, seg = q >> 1, odd = q & 1;
        int ch = seg * 128 + 2 * lane + odd;
        unsigned a = wrbf[(2 * kp) * 384 + ch], b = wrbf[(2 * kp + 1) * 384 + ch];
        wr[i] = a | (b << 16);
    }
}

DI void gemm_wave(const u16* __restrict__ sv, const bf16x8* __restrict__ w1f, const bf16x8* __restrict__ w2f,
                  u16* __restrict__ phi, int tile, int N, int lane) {
    const int r = lane & 31, h = lane >> 5;
    const int node = tile * 32 + r;
    const bool valid = node < N;
    const bf16x8* srow = (const bf16x8*)(sv + (size_t)(valid ? node : 0) * 512);
    bf16x8 sf[8];
#pragma unroll
    for (int ks = 0; ks < 8; ++ks) sf[ks] = srow[2 * ks + h];
    bf16x8 hf[8];
#pragma unroll
    for (int t = 0; t < 4; ++t) {
        f32x16 acc;
#pragma unroll
        for (int i = 0; i < 16; ++i) acc[i] = 0.f;
#pragma unroll
        for (int ks = 0; ks < 8; ++ks)
            acc = __builtin_amdgcn_mfma_f32_32x32x16_bf16(w1f[(t * 8 + ks) * 64 + lane], sf[ks], acc, 0, 0, 0);
        unsigned p[8];
#pragma unroll
        for (int i = 0; i < 8; ++i) {
            float x0 = rb(acc[2 * i]), x1 = rb(acc[2 * i + 1]);
            float y0 = x0 / (1.0f + __expf(-x0)), y1 = x1 / (1.0f + __expf(-x1));
            p[i] = pack2(y0, y1);
        }
        u32x4 a = {p[0], p[1], p[2], p[3]}, b = {p[4], p[5], p[6], p[7]};
        hf[2 * t] = __builtin_bit_cast(bf16x8, a);
        hf[2 * t + 1] = __builtin_bit_cast(bf16x8, b);
    }
    u16* prow = phi + (size_t)node * 384 + 4 * h;
#pragma unroll 2
    for (int c = 0; c < 12; ++c) {
        f32x16 acc;
#pragma unroll
        for (int i = 0; i < 16; ++i) acc[i] = 0.f;
#pragma unroll
        for (int ks = 0; ks < 8; ++ks)
            acc = __builtin_amdgcn_mfma_f32_32x32x16_bf16(w2f[(c * 8 + ks) * 64 + lane], hf[ks], acc, 0, 0, 0);
        if (valid) {
#pragma unroll
            for (int g = 0; g < 4; ++g) {
                uint2 o;
                o.x = pack2(acc[4 * g], acc[4 * g + 1]);
                o.y = pack2(acc[4 * g + 2], acc[4 * g + 3]);
                *(uint2*)(prow + 32 * c + 8 * g) = o;
            }
        }
    }
}

__global__ __launch_bounds__(256) void k_node_edge(const u16* __restrict__ sv, const u16* __restrict__ pos,
                                                   const int* __restrict__ src, const int* __restrict__ dst,
                                                   const u16* __restrict__ freqs, const bf16x8* __restrict__ w1f,
                                                   const bf16x8* __restrict__ w2f, u16* __restrict__ phi,
                                                   int* __restrict__ cnt, int2* __restrict__ slots,
                                                   u32x4* __restrict__ entries, int N, int E, int cap,
                                                   int gemm_blocks) {
    if ((int)blockIdx.x < gemm_blocks) {
        const int lane = threadIdx.x & 63;
        const int tile = blockIdx.x * 4 + (threadIdx.x >> 6);
        if (tile * 32 < N) gemm_wave(sv, w1f, w2f, phi, tile, N, lane);
        return;
    }
    const int e = ((int)blockIdx.x - gemm_blocks) * 256 + threadIdx.x;
    if (e >= E) return;
    const int s = src[e], t = dst[e];
    float rx, ry, rz, d;
    if (!edge_dist(pos, s, t, rx, ry, rz, d)) return;
    const int slot = atomicAdd(&cnt[t], 1);
    if (slot >= cap) return;
    EdgeGeom g;
    edge_rest(freqs, rx, ry, rz, d, g);
    const size_t idx = (size_t)t * cap + slot;
    slots[idx] = make_int2(e, s);
    u32x4 a = {g.rbf[0], g.rbf[1], g.rbf[2], g.rbf[3]};
    u32x4 b = {g.rbf[4], g.rbf[5], g.rbf[6], g.rbf[7]};
    u32x4 c = {__float_as_uint(g.fcut), __float_as_uint(g.rx), __float_as_uint(g.ry), __float_as_uint(g.rz)};
    entries[idx * 4 + 0] = a;
    entries[idx * 4 + 1] = b;
    entries[idx * 4 + 2] = c;
}

struct RowData { unsigned ps, pvv, pvr, vx, vy, vz; };

DI void load_rows(const u16* __restrict__ sv, const u16* __restrict__ phi, int s, int lane, RowData& r) {
    const unsigned* p = (const unsigned*)(phi + (size_t)s * 384) + lane;
    const unsigned* v = (const unsigned*)(sv + (size_t)s * 512) + lane;
    r.ps = p[0]; r.pvv = p[64]; r.pvr = p[128];
    r.vx = v[64]; r.vy = v[128]; r.vz = v[192];
}

DI float dot2(unsigned a, unsigned b, float c) {
    return __builtin_amdgcn_fdot2_f32_bf16(__builtin_bit_cast(bf16x2_t, a), __builtin_bit_cast(bf16x2_t, b), c, false);
}

DI void accum_edge(const RowData& r, const unsigned (&rbf)[8], float fcut, float hx, float hy, float hz,
                   const unsigned (&w)[48], float (&acc)[8]) {
    float wf[6];
#pragma unroll
    for (int q = 0; q < 6; ++q) wf[q] = 0.f;
#pragma unroll
    for (int kp = 0; kp < 8; ++kp)
#pragma unroll
        for (int q = 0; q < 6; ++q) wf[q] = dot2(rbf[kp], w[kp * 6 + q], wf[q]);
#pragma unroll
    for (int q = 0; q < 6; ++q) wf[q] *= fcut;
    acc[0] += lo16(r.ps) * wf[0];
    acc[1] += hi16(r.ps) * wf[1];
    float mvv0 = lo16(r.pvv) * wf[2], mvv1 = hi16(r.pvv) * wf[3];
    float mvr0 = lo16(r.pvr) * wf[4], mvr1 = hi16(r.pvr) * wf[5];
    acc[2] += mvv0 * lo16(r.vx) + mvr0 * hx;
    acc[3] += mvv1 * hi16(r.vx) + mvr1 * hx;
    acc[4] += mvv0 * lo16(r.vy) + mvr0 * hy;
    acc[5] += mvv1 * hi16(r.vy) + mvr1 * hy;
    acc[6] += mvv0 * lo16(r.vz) + mvr0 * hz;
    acc[7] += mvv1 * hi16(r.vz) + mvr1 * hz;
}

__global__ __launch_bounds__(256) void k_gather(const u16* __restrict__ sv, const u16* __restrict__ phi,
                                                const int* __restrict__ cnt, const int2* __restrict__ slots,
                                                const u32x4* __restrict__ entries, const unsigned* __restrict__ wr,
                                                u16* __restrict__ out, int N, int cap,
                                                const u16* __restrict__ pos, const int* __restrict__ src,
                                                const int* __restrict__ dst, const u16* __restrict__ freqs, int E) {
    const int lane = threadIdx.x & 63;
    const int wave = __builtin_amdgcn_readfirstlane(blockIdx.x * 4 + (threadIdx.x >> 6));
    const int nw = gridDim.x * 4;
    unsigned w[48];
#pragma unroll
    for (int j = 0; j < 48; ++j) w[j] = wr[j * 64 + lane];

    int node = wave;
    int c_next = 0;
    int2 sl_next = make_int2(0, 0);
    if (node < N) {
        c_next = cnt[node];
        if (lane < 16) sl_next = slots[(size_t)node * cap + lane];
    }
    for (; node < N; node += nw) {
        const int c = __builtin_amdgcn_readfirstlane(c_next);
        int2 sl = sl_next;
        const int nn = node + nw;
        if (nn < N) {
            c_next = cnt[nn];
            if (lane < 16) sl_next = slots[(size_t)nn * cap + lane];
        }
        const unsigned* own = (const unsigned*)(sv + (size_t)node * 512) + lane;
        unsigned o0 = own[0], o1 = own[64], o2 = own[128], o3 = own[192];
        float acc[8];
#pragma unroll
        for (int i = 0; i < 8; ++i) acc[i] = 0.f;

        if (c <= cap) {
            if (c > 16) {
                if (lane < c) sl = slots[(size_t)node * cap + lane];
            }
            int rank = 0;
            for (int j = 0; j < c; ++j) {
                int ej = __builtin_amdgcn_readlane(sl.x, j);
                rank += (ej < sl.x) ? 1 : 0;
            }
            for (int base = 0; base < c; base += 4) {
                RowData rd[4];
                u32x4 ea[4], eb[4], ec[4];
#pragma unroll
                for (int u = 0; u < 4; ++u) {
                    if (base + u < c) {
                        unsigned long long m = __ballot(lane < c && rank == base + u);
                        int idx = __builtin_ctzll(m);
                        int s = __builtin_amdgcn_readlane(sl.y, idx);
                        load_rows(sv, phi, s, lane, rd[u]);
                        const u32x4* ep = entries + ((size_t)node * cap + idx) * 4;
                        ea[u] = ep[0]; eb[u] = ep[1]; ec[u] = ep[2];
                    }
                }
#pragma unroll
                for (int u = 0; u < 4; ++u) {
                    if (base + u < c) {
                        unsigned rbf[8] = {ea[u][0], ea[u][1], ea[u][2], ea[u][3], eb[u][0], eb[u][1], eb[u][2], eb[u][3]};
                        accum_edge(rd[u], rbf, __uint_as_float(ec[u][0]), __uint_as_float(ec[u][1]),
                                   __uint_as_float(ec[u][2]), __uint_as_float(ec[u][3]), w, acc);
                    }
                }
            }
        } else {
            for (int base = 0; base < E; base += 64) {
                int e = base + lane;
                bool match = (e < E) && (dst[e] == node);
                unsigned long long m = __ballot(match);
                while (m) {
                    int l = __builtin_ctzll(m);
                    m &= m - 1;
                    int ee = base + l;
                    int s = src[ee];
                    float rx, ry, rz, d;
                    if (edge_dist(pos, s, node, rx, ry, rz, d)) {
                        EdgeGeom g;
                        edge_rest(freqs, rx, ry, rz, d, g);
                        RowData rd;
                        load_rows(sv, phi, s, lane, rd);
                        accum_edge(rd, g.rbf, g.fcut, g.rx, g.ry, g.rz, w, acc);
                    }
                }
            }
        }
        unsigned* op = (unsigned*)(out + (size_t)node * 512) + lane;
        op[0] = pack2(lo16(o0) + acc[0], hi16(o0) + acc[1]);
        op[64] = pack2(lo16(o1) + acc[2], hi16(o1) + acc[3]);
        op[128] = pack2(lo16(o2) + acc[4], hi16(o2) + acc[5]);
        op[192] = pack2(lo16(o3) + acc[6], hi16(o3) + acc[7]);
    }
}

static inline size_t align_up(size_t x, size_t a) { return (x + a - 1) / a * a; }

extern "C" void kernel_launch(void* const* d_in, const int* in_sizes, int n_in, void* d_out, int out_size,
                              void* d_ws, size_t ws_size, hipStream_t stream) {
    const u16* sv = (const u16*)d_in[0];
    const u16* pos = (const u16*)d_in[1];
    const int* src = (const int*)d_in[2];
    const int* dst = (const int*)d_in[3];
    const u16* freqs = (const u16*)d_in[4];
    const u16* w1 = (const u16*)d_in[5];
    const u16* w2 = (const u16*)d_in[6];
    const u16* wrbf = (const u16*)d_in[7];
    u16* out = (u16*)d_out;
    const int N = in_sizes[1] / 3;
    const int E = in_sizes[2];

    int cap = 64;
    size_t off_cnt, off_w1f, off_w2f, off_wr, off_phi, off_slots, off_ent, total;
    for (;;) {
        size_t o = 0;
        off_cnt = o; o = align_up(o + (size_t)N * 4, 256);
        off_w1f = o; o = align_up(o + 128 * 128 * 2, 256);
        off_w2f = o; o = align_up(o + 128 * 384 * 2, 256);
        off_wr = o; o = align_up(o + 48 * 64 * 4, 256);
        off_phi = o; o = align_up(o + (size_t)N * 384 * 2, 256);
        off_slots = o; o = align_up(o + (size_t)N * cap * 8, 256);
        off_ent = o; o = align_up(o + (size_t)N * cap * 64, 256);
        total = o;
        if (total <= ws_size || cap <= 16) break;
        cap >>= 1;
    }
    char* ws = (char*)d_ws;
    int* cnt = (int*)(ws + off_cnt);
    u16* w1f = (u16*)(ws + off_w1f);
    u16* w2f = (u16*)(ws + off_w2f);
    unsigned* wr = (unsigned*)(ws + off_wr);
    u16* phi = (u16*)(ws + off_phi);
    int2* slots = (int2*)(ws + off_slots);
    u32x4* entries = (u32x4*)(ws + off_ent);

    k_prep<<<128, 256, 0, stream>>>(w1, w2, wrbf, cnt, w1f, w2f, wr, N);
    const int tiles = (N + 31) / 32;
    const int gemm_blocks = (tiles + 3) / 4;
    const int edge_blocks = (E + 255) / 256;
    k_node_edge<<<gemm_blocks + edge_blocks, 256, 0, stream>>>(sv, pos, src, dst, freqs, (const bf16x8*)w1f,
                                                              (const bf16x8*)w2f, phi, cnt, slots, entries, N, E,
                                                              cap, gemm_blocks);
    int gblocks = (N + 3) / 4;
    if (gblocks > 1280) gblocks = 1280;
    k_gather<<<gblocks, 256, 0, stream>>>(sv, phi, cnt, slots, entries, wr, out, N, cap, pos, src, dst, freqs, E);
}
```

```cpp
#include <hip/hip_runtime.h>
#include <hip/hip_bf16.h>
#include <stdint.h>

typedef __attribute__((ext_vector_type(8))) short bf16x8;
typedef __attribute__((ext_vector_type(16))) float f32x16;
typedef __attribute__((ext_vector_type(4))) unsigned u32x4;
typedef __attribute__((ext_vector_type(2))) __bf16 bf16x2_t;
typedef __attribute__((ext_vector_type(2))) float f32x2_t;
typedef unsigned short u16;

#define DI __device__ __forceinline__

DI float bf2f(u16 x) { return __uint_as_float(((unsigned)x) << 16); }
DI unsigned pack2(float a, float b) {
    f32x2_t v = {a, b};
    bf16x2_t r = __builtin_convertvector(v, bf16x2_t);
    return __builtin_bit_cast(unsigned, r);
}
DI float rb(float a) {
    return __uint_as_float(pack2(a, 0.f) << 16);
}
DI float lo16(unsigned u) { return __uint_as_float(u << 16); }
DI float hi16(unsigned u) { return __uint_as_float(u & 0xffff0000u); }

struct EdgeGeom {
    unsigned rbf[8];
    float fcut, rx, ry, rz;
};

DI bool edge_dist(const u16* __restrict__ pos, int s, int t, float& rx, float& ry, float& rz, float& d) {
    float sx = bf2f(pos[3 * s]), sy = bf2f(pos[3 * s + 1]), sz = bf2f(pos[3 * s + 2]);
    float tx = bf2f(pos[3 * t]), ty = bf2f(pos[3 * t + 1]), tz = bf2f(pos[3 * t + 2]);
    rx = rb(sx - tx); ry = rb(sy - ty); rz = rb(sz - tz);
    float q = rb(rb(rx * rx) + rb(ry * ry) + rb(rz * rz));
    d = rb(sqrtf(rb(q + 1e-08f)));
    return d < 5.0f;
}
DI void edge_rest(const unsigned (&fq)[8], float fmax, float rx, float ry, float rz, float d, EdgeGeom& g) {
    const float inv = 1.0f / d;
    g.rx = rb(rx * inv); g.ry = rb(ry * inv); g.rz = rb(rz * inv);
    const float ca = rb(rb(3.141592653589793f * d) / 5.0f);
    float c = rb(__builtin_amdgcn_cosf(ca * 0.15915494309189535f));
    g.fcut = rb(0.5f * rb(c + 1.0f));
    if (d * fmax <= 60.0f) {
#pragma unroll
        for (int kp = 0; kp < 8; ++kp) {
            float x0 = rb(d * lo16(fq[kp])), x1 = rb(d * hi16(fq[kp]));
            float a0 = rb(rb(__builtin_amdgcn_sinf(x0 * 0.15915494309189535f)) * inv);
            float a1 = rb(rb(__builtin_amdgcn_sinf(x1 * 0.15915494309189535f)) * inv);
            g.rbf[kp] = pack2(a0, a1);
        }
    } else {
#pragma unroll
        for (int kp = 0; kp < 8; ++kp) {
            float x0 = rb(d * lo16(fq[kp])), x1 = rb(d * hi16(fq[kp]));
            float a0 = rb(rb(sinf(x0)) * inv);
            float a1 = rb(rb(sinf(x1)) * inv);
            g.rbf[kp] = pack2(a0, a1);
        }
    }
}

typedef __attribute__((ext_vector_type(4))) short s16x4;
typedef __attribute__((address_space(3))) s16x4 lds_s16x4;

constexpr int W1_STRIDE = 256;
constexpr int W2_STRIDE = 768;
constexpr int W2_BASE = 128 * W1_STRIDE;
constexpr int STG_BASE = W2_BASE + 128 * W2_STRIDE;
constexpr int FRONT_LDS = STG_BASE + 4 * 32 * 144;

DI bf16x8 tr_frag(const char* lds, int off0, int off1) {
    s16x4 a = __builtin_amdgcn_ds_read_tr16_b64_v4i16((lds_s16x4*)(lds + off0));
    s16x4 b = __builtin_amdgcn_ds_read_tr16_b64_v4i16((lds_s16x4*)(lds + off1));
    return __builtin_shufflevector(a, b, 0, 1, 2, 3, 4, 5, 6, 7);
}

__global__ __launch_bounds__(512) void k_front(const u16* __restrict__ sv, const u16* __restrict__ w1,
                                               const u16* __restrict__ w2, const u16* __restrict__ wrbf,
                                               u16* __restrict__ phi, unsigned* __restrict__ wr,
                                               const u16* __restrict__ pos, const int* __restrict__ src,
                                               const int* __restrict__ dst, const u16* __restrict__ freqs,
                                               int* __restrict__ cnt, int4* __restrict__ slots,
                                               u32x4* __restrict__ entries, int4* __restrict__ ovf_meta,
                                               int N, int E, int cap) {
    __shared__ __attribute__((aligned(16))) char lds[FRONT_LDS];
    const int tid = threadIdx.x;
    const int lane = tid & 63;
    const int wv = __builtin_amdgcn_readfirstlane(tid >> 6);
    const int r = lane & 31, h = lane >> 5;
    const int tile = blockIdx.x * 4 + (wv & 3);
    const int node = tile * 32 + r;
    const bool valid = node < N;
    bf16x8 sf[8];
    if (wv < 4) {
        const bf16x8* srow = (const bf16x8*)(sv + (size_t)(valid ? node : 0) * 512);
#pragma unroll
        for (int ks = 0; ks < 8; ++ks) sf[ks] = srow[2 * ks + h];
    }
    {
        const u32x4* g1 = (const u32x4*)w1;
#pragma unroll
        for (int i = 0; i < 4; ++i) {
            int c = i * 512 + tid;
            int row = c >> 4, ch = c & 15;
            *(u32x4*)(lds + row * W1_STRIDE + ((ch * 16) ^ ((row & 3) << 6))) = g1[c];
        }
        const u32x4* g2 = (const u32x4*)w2;
#pragma unroll
        for (int i = 0; i < 12; ++i) {
            int c = i * 512 + tid;
            int row = c / 48, ch = c % 48;
            *(u32x4*)(lds + W2_BASE + row * W2_STRIDE + ((ch * 16) ^ ((row & 3) << 6))) = g2[c];
        }
    }
    __syncthreads();

    if (wv >= 4) {
        const int ew = blockIdx.x * 4 + (wv - 4);
        const int nw = gridDim.x * 4;
        const int nchunks = (E + 63) >> 6;
        unsigned fq[8];
#pragma unroll
        for (int kp = 0; kp < 8; ++kp) fq[kp] = (unsigned)freqs[2 * kp] | ((unsigned)freqs[2 * kp + 1] << 16);
        for (int i = ew * 64 + lane; i < 48 * 64; i += nw * 64) {
            int half = i & 1, ln = (i >> 1) & 63, gq = i >> 7;
            int g = gq >> 2, q = gq & 3;
            int ch = (g >> 1) * 128 + 2 * ln + (g & 1);
            int k = 4 * q + 2 * half;
            unsigned a = wrbf[k * 384 + ch], b = wrbf[(k + 1) * 384 + ch];
            wr[i] = a | (b << 16);
        }
        float fmax = 0.f;
#pragma unroll
        for (int kp = 0; kp < 8; ++kp) fmax = fmaxf(fmax, fmaxf(fabsf(lo16(fq[kp])), fabsf(hi16(fq[kp]))));
        constexpr int EU = 8;
        for (int c0 = ew; c0 < nchunks; c0 += nw * EU) {
            int es[EU], ss[EU], ts[EU];
#pragma unroll
            for (int u = 0; u < EU; ++u) {
                int e = (c0 + u * nw) * 64 + lane;
                es[u] = e;
                int ec = e < E ? e : E - 1;
                ss[u] = src[ec]; ts[u] = dst[ec];
            }
            float rx[EU], ry[EU], rz[EU], d[EU];
            bool act[EU];
#pragma unroll
            for (int u = 0; u < EU; ++u) act[u] = edge_dist(pos, ss[u], ts[u], rx[u], ry[u], rz[u], d[u]) && es[u] < E;
            int slot[EU];
#pragma unroll
            for (int u = 0; u < EU; ++u) slot[u] = act[u] ? atomicAdd(&cnt[ts[u]], 1) : 0;
#pragma unroll
            for (int u = 0; u < EU; ++u) {
                const unsigned long long m = __ballot(act[u]);
                if (act[u]) {
                    const int eidx = (es[u] - lane) + __builtin_popcountll(m & ((1ull << lane) - 1ull));
                    EdgeGeom g;
                    edge_rest(fq, fmax, rx[u], ry[u], rz[u], d[u], g);
                    u32x4* ep = entries + (size_t)eidx * 4;
                    u32x4 a = {g.rbf[0], g.rbf[1], g.rbf[2], g.rbf[3]};
                    u32x4 b = {g.rbf[4], g.rbf[5], g.rbf[6], g.rbf[7]};
                    u32x4 c = {__float_as_uint(g.fcut), __float_as_uint(g.rx), __float_as_uint(g.ry),
                               __float_as_uint(g.rz)};
                    ep[0] = a;
                    ep[1] = b;
                    ep[2] = c;
                    if (slot[u] < cap) {
                        slots[(size_t)ts[u] * cap + slot[u]] = make_int4(es[u], ss[u], eidx, ts[u]);
                    } else {
                        const int o = atomicAdd(&cnt[N], 1);
                        ovf_meta[o] = make_int4(es[u], ss[u], eidx, ts[u]);
                    }
                }
            }
        }
        return;
    }

    const int i16 = lane & 15, q = i16 >> 2, p4 = i16 & 3, g1b = (lane >> 4) & 1;
    const int colb = ((16 * g1b + 4 * p4) * 2) ^ (q << 6);
    bf16x8 hf[8];
#pragma unroll
    for (int t = 0; t < 4; ++t) {
        bf16x8 fr[8];
#pragma unroll
        for (int ks = 0; ks < 8; ++ks) {
            int off = (16 * ks + 8 * h + q) * W1_STRIDE + ((64 * t) ^ colb);
            fr[ks] = tr_frag(lds, off, off + 4 * W1_STRIDE);
        }
        f32x16 acc;
#pragma unroll
        for (int i = 0; i < 16; ++i) acc[i] = 0.f;
#pragma unroll
        for (int ks = 0; ks < 8; ++ks) acc = __builtin_amdgcn_mfma_f32_32x32x16_bf16(fr[ks], sf[ks], acc, 0, 0, 0);
        unsigned p[8];
#pragma unroll
        for (int i = 0; i < 8; ++i) {
            float x0 = rb(acc[2 * i]), x1 = rb(acc[2 * i + 1]);
            float y0 = x0 * __builtin_amdgcn_rcpf(1.0f + __expf(-x0)), y1 = x1 * __builtin_amdgcn_rcpf(1.0f + __expf(-x1));
            p[i] = pack2(y0, y1);
        }
        u32x4 a = {p[0], p[1], p[2], p[3]}, b = {p[4], p[5], p[6], p[7]};
        hf[2 * t] = __builtin_bit_cast(bf16x8, a);
        hf[2 * t + 1] = __builtin_bit_cast(bf16x8, b);
    }
    char* stg = lds + STG_BASE + wv * (32 * 144);
    const int rrow = lane >> 3, rch = lane & 7;
    bf16x8 fa[8], fb[8];
#pragma unroll
    for (int ks = 0; ks < 8; ++ks) {
        int off = W2_BASE + (16 * ks + 4 * h + q) * W2_STRIDE + colb;
        fa[ks] = tr_frag(lds, off, off + 8 * W2_STRIDE);
    }
#pragma unroll
    for (int c2 = 0; c2 < 6; ++c2) {
        {
            const int c = 2 * c2 + 1;
#pragma unroll
            for (int ks = 0; ks < 8; ++ks) {
                int off = W2_BASE + (16 * ks + 4 * h + q) * W2_STRIDE + (c >> 2) * 256 + (((c & 3) * 64) ^ colb);
                fb[ks] = tr_frag(lds, off, off + 8 * W2_STRIDE);
            }
        }
        {
            f32x16 acc;
#pragma unroll
            for (int i = 0; i < 16; ++i) acc[i] = 0.f;
#pragma unroll
            for (int ks = 0; ks < 8; ++ks) acc = __builtin_amdgcn_mfma_f32_32x32x16_bf16(fa[ks], hf[ks], acc, 0, 0, 0);
#pragma unroll
            for (int g = 0; g < 4; ++g) {
                uint2 o;
                o.x = pack2(acc[4 * g], acc[4 * g + 1]);
                o.y = pack2(acc[4 * g + 2], acc[4 * g + 3]);
                *(uint2*)(stg + r * 144 + 16 * g + 8 * h) = o;
            }
        }
        if (c2 < 5) {
            const int c = 2 * c2 + 2;
#pragma unroll
            for (int ks = 0; ks < 8; ++ks) {
                int off = W2_BASE + (16 * ks + 4 * h + q) * W2_STRIDE + (c >> 2) * 256 + (((c & 3) * 64) ^ colb);
                fa[ks] = tr_frag(lds, off, off + 8 * W2_STRIDE);
            }
        }
        {
            f32x16 acc;
#pragma unroll
            for (int i = 0; i < 16; ++i) acc[i] = 0.f;
#pragma unroll
            for (int ks = 0; ks < 8; ++ks) acc = __builtin_amdgcn_mfma_f32_32x32x16_bf16(fb[ks], hf[ks], acc, 0, 0, 0);
#pragma unroll
            for (int g = 0; g < 4; ++g) {
                uint2 o;
                o.x = pack2(acc[4 * g], acc[4 * g + 1]);
                o.y = pack2(acc[4 * g + 2], acc[4 * g + 3]);
                *(uint2*)(stg + r * 144 + 64 + 16 * g + 8 * h) = o;
            }
        }
        __builtin_amdgcn_wave_barrier();
        u32x4 v[4];
#pragma unroll
        for (int i = 0; i < 4; ++i) v[i] = *(const u32x4*)(stg + (rrow + 8 * i) * 144 + rch * 16);
#pragma unroll
        for (int i = 0; i < 4; ++i) {
            int nd = tile * 32 + rrow + 8 * i;
            if (nd < N) *(u32x4*)(phi + (size_t)nd * 384 + c2 * 64 + rch * 8) = v[i];
        }
        __builtin_amdgcn_wave_barrier();
    }
}

struct RowData { unsigned ps, pvv, pvr, vx, vy, vz; };

DI void load_rows(const u16* __restrict__ sv, const u16* __restrict__ phi, int s, int lane, RowData& r) {
    const unsigned* p = (const unsigned*)(phi + (size_t)s * 384) + lane;
    const unsigned* v = (const unsigned*)(sv + (size_t)s * 512) + lane;
    r.ps = p[0]; r.pvv = p[64]; r.pvr = p[128];
    r.vx = v[64]; r.vy = v[128]; r.vz = v[192];
}

typedef __attribute__((ext_vector_type(4))) float f32x4;
typedef __attribute__((ext_vector_type(2))) unsigned u32x2;

DI void accum_edge(const RowData& r, const float (&wf)[6], float hx, float hy, float hz, float (&acc)[8]) {
    acc[0] += lo16(r.ps) * wf[0];
    acc[1] += hi16(r.ps) * wf[1];
    float mvv0 = lo16(r.pvv) * wf[2], mvv1 = hi16(r.pvv) * wf[3];
    float mvr0 = lo16(r.pvr) * wf[4], mvr1 = hi16(r.pvr) * wf[5];
    acc[2] += mvv0 * lo16(r.vx) + mvr0 * hx;
    acc[3] += mvv1 * hi16(r.vx) + mvr1 * hx;
    acc[4] += mvv0 * lo16(r.vy) + mvr0 * hy;
    acc[5] += mvv1 * hi16(r.vy) + mvr1 * hy;
    acc[6] += mvv0 * lo16(r.vz) + mvr0 * hz;
    acc[7] += mvv1 * hi16(r.vz) + mvr1 * hz;
}

DI void wf_mfma(const u32x4& a0, const u32x4& a1, const u32x2* __restrict__ wl, int lane, f32x4 (&D)[6]) {
    const u32x2 aq[4] = {{a0[0], a0[1]}, {a0[2], a0[3]}, {a1[0], a1[1]}, {a1[2], a1[3]}};
#pragma unroll
    for (int g = 0; g < 6; ++g) {
        f32x4 d = {0.f, 0.f, 0.f, 0.f};
#pragma unroll
        for (int q = 0; q < 4; ++q)
            d = __builtin_amdgcn_mfma_f32_4x4x4bf16_1k(__builtin_bit_cast(s16x4, aq[q]),
                                                      __builtin_bit_cast(s16x4, wl[(g * 4 + q) * 64 + lane]), d, 0, 0, 0);
        D[g] = d;
    }
}

struct Chunk {
    u32x4 a0, a1, a2;
    RowData rd[4];
};

DI void chunk_load(const u16* __restrict__ sv, const u16* __restrict__ phi, const u32x4* __restrict__ entries,
                   int lane, int c, int base, int rank, int sly, int slz, Chunk& ck) {
    int idx[4], sr[4];
#pragma unroll
    for (int u = 0; u < 4; ++u) {
        const int rr = base + u < c ? base + u : base;
        const int ln = __builtin_ctzll(__ballot(lane < c && rank == rr) | (1ull << 63));
        sr[u] = __builtin_amdgcn_readlane(sly, ln);
        idx[u] = __builtin_amdgcn_readlane(slz, ln);
    }
    const int u4 = lane & 3;
    int myidx = idx[0];
    myidx = u4 == 1 ? idx[1] : myidx;
    myidx = u4 == 2 ? idx[2] : myidx;
    myidx = u4 == 3 ? idx[3] : myidx;
    const u32x4* ep = entries + (size_t)myidx * 4;
    ck.a0 = ep[0]; ck.a1 = ep[1]; ck.a2 = ep[2];
#pragma unroll
    for (int u = 0; u < 4; ++u) load_rows(sv, phi, sr[u], lane, ck.rd[u]);
}

DI void chunk_compute(const Chunk& ck, const u32x2* __restrict__ wl, int lane, int c, int base, float (&acc)[8]) {
    f32x4 D[6];
    int lofs = lane;
    asm volatile("" : "+v"(lofs));
    wf_mfma(ck.a0, ck.a1, wl, lofs, D);
#pragma unroll
    for (int u = 0; u < 4; ++u) {
        if (u == 0 || base + u < c) {
            const float fc = __uint_as_float(__builtin_amdgcn_readlane(ck.a2[0], u));
            const float hx = __uint_as_float(__builtin_amdgcn_readlane(ck.a2[1], u));
            const float hy = __uint_as_float(__builtin_amdgcn_readlane(ck.a2[2], u));
            const float hz = __uint_as_float(__builtin_amdgcn_readlane(ck.a2[3], u));
            float wf[6];
#pragma unroll
            for (int g = 0; g < 6; ++g) wf[g] = D[g][u] * fc;
            accum_edge(ck.rd[u], wf, hx, hy, hz, acc);
        }
    }
}

#define GOCC 1
__global__ __launch_bounds__(256, GOCC) void k_gather(const u16* __restrict__ sv, const u16* __restrict__ phi,
                                                const int* __restrict__ cnt, const int4* __restrict__ slots,
                                                const u32x4* __restrict__ entries, const u32x4* __restrict__ wr,
                                                const int4* __restrict__ ovf_meta,
                                                u16* __restrict__ out, int N, int cap) {
    __shared__ __attribute__((aligned(16))) u32x4 wlds[768];
    const int lane = threadIdx.x & 63;
    const int wave = __builtin_amdgcn_readfirstlane(blockIdx.x * 4 + (threadIdx.x >> 6));
    const int nw = gridDim.x * 4;
#pragma unroll
    for (int i = 0; i < 3; ++i) wlds[i * 256 + threadIdx.x] = wr[i * 256 + threadIdx.x];
    const u32x2* wl = (const u32x2*)wlds;

    int node = wave;
    const int n0 = node < N ? node : N - 1;
    int c_next = cnt[n0];
    int4 sl_next = slots[(size_t)n0 * cap + (lane & 15)];
    __syncthreads();
    bool any_big = false;
    for (; node < N; node += nw) {
        const int c = __builtin_amdgcn_readfirstlane(c_next);
        const int4 sl = sl_next;
        const int nn = node + nw < N ? node + nw : N - 1;
        const unsigned* own = (const unsigned*)(sv + (size_t)node * 512) + lane;
        unsigned* op = (unsigned*)(out + (size_t)node * 512) + lane;
        if (c == 0 || c > 16) {
            if (c == 0) {
                const unsigned o0 = own[0], o1 = own[64], o2 = own[128], o3 = own[192];
                c_next = cnt[nn];
                sl_next = slots[(size_t)nn * cap + (lane & 15)];
                op[0] = o0; op[64] = o1; op[128] = o2; op[192] = o3;
            } else {
                any_big = true;
                c_next = cnt[nn];
                sl_next = slots[(size_t)nn * cap + (lane & 15)];
            }
            continue;
        }
        float acc[8];
#pragma unroll
        for (int i = 0; i < 8; ++i) acc[i] = 0.f;
        int rank = 0;
        for (int j = 0; j < c; ++j) {
            int ej = __builtin_amdgcn_readlane(sl.x, j);
            rank += (ej < sl.x) ? 1 : 0;
        }
        unsigned o0, o1, o2, o3;
        {
            Chunk ck;
            chunk_load(sv, phi, entries, lane, c, 0, rank, sl.y, sl.z, ck);
            o0 = own[0]; o1 = own[64]; o2 = own[128]; o3 = own[192];
            c_next = cnt[nn];
            sl_next = slots[(size_t)nn * cap + (lane & 15)];
            chunk_compute(ck, wl, lane, c, 0, acc);
        }
        for (int base = 4; base < c; base += 4) {
            Chunk ck;
            chunk_load(sv, phi, entries, lane, c, base, rank, sl.y, sl.z, ck);
            chunk_compute(ck, wl, lane, c, base, acc);
        }
        op[0] = pack2(lo16(o0) + acc[0], hi16(o0) + acc[1]);
        op[64] = pack2(lo16(o1) + acc[2], hi16(o1) + acc[3]);
        op[128] = pack2(lo16(o2) + acc[4], hi16(o2) + acc[5]);
        op[192] = pack2(lo16(o3) + acc[6], hi16(o3) + acc[7]);
    }
    if (!any_big) return;

    for (node = wave; node < N; node += nw) {
        const int c = __builtin_amdgcn_readfirstlane(cnt[node]);
        if (c <= 16) continue;
        int4 sl = make_int4(0, 0, 0, 0);
        if (lane < cap) sl = slots[(size_t)node * cap + lane];
        const unsigned* own = (const unsigned*)(sv + (size_t)node * 512) + lane;
        unsigned o0 = own[0], o1 = own[64], o2 = own[128], o3 = own[192];
        float acc[8];
#pragma unroll
        for (int i = 0; i < 8; ++i) acc[i] = 0.f;
        const int L = (c > cap) ? cnt[N] : 0;
        const int nb = c < cap ? c : cap;
        int last = -1;
        for (int it = 0; it < c; ++it) {
            int best = 0x7fffffff, loc = 0, bs = 0;
            if (lane < nb && sl.x > last) { best = sl.x; loc = sl.z; bs = sl.y; }
            for (int i = lane; i < L; i += 64) {
                int4 o = ovf_meta[i];
                if (o.w == node && o.x > last && o.x < best) { best = o.x; loc = o.z; bs = o.y; }
            }
            int mn = best;
#pragma unroll
            for (int sft = 32; sft >= 1; sft >>= 1) {
                int other = __shfl_xor(mn, sft, 64);
                mn = other < mn ? other : mn;
            }
            unsigned long long m = __ballot(best == mn);
            int owner = __builtin_ctzll(m);
            int l2 = __builtin_amdgcn_readlane(loc, owner);
            int s = __builtin_amdgcn_readlane(bs, owner);
            const u32x4* ep = entries + (size_t)l2 * 4;
            RowData rd;
            load_rows(sv, phi, s, lane, rd);
            u32x4 a0 = ep[0], a1 = ep[1], a2 = ep[2];
            f32x4 D[6];
            int lofs = lane;
            asm volatile("" : "+v"(lofs));
            wf_mfma(a0, a1, wl, lofs, D);
            float wf[6];
#pragma unroll
            for (int g = 0; g < 6; ++g) wf[g] = D[g][0] * __uint_as_float(a2[0]);
            accum_edge(rd, wf, __uint_as_float(a2[1]), __uint_as_float(a2[2]), __uint_as_float(a2[3]), acc);
            last = mn;
        }
        unsigned* op = (unsigned*)(out + (size_t)node * 512) + lane;
        op[0] = pack2(lo16(o0) + acc[0], hi16(o0) + acc[1]);
        op[64] = pack2(lo16(o1) + acc[2], hi16(o1) + acc[3]);
        op[128] = pack2(lo16(o2) + acc[4], hi16(o2) + acc[5]);
        op[192] = pack2(lo16(o3) + acc[6], hi16(o3) + acc[7]);
    }
}

static inline size_t align_up(size_t x, size_t a) { return (x + a - 1) / a * a; }

#define GBLOCKS 1280

extern "C" void kernel_launch(void* const* d_in, const int* in_sizes, int n_in, void* d_out, int out_size,
                              void* d_ws, size_t ws_size, hipStream_t stream) {
    const u16* sv = (const u16*)d_in[0];
    const u16* pos = (const u16*)d_in[1];
    const int* src = (const int*)d_in[2];
    const int* dst = (const int*)d_in[3];
    const u16* freqs = (const u16*)d_in[4];
    const u16* w1 = (const u16*)d_in[5];
    const u16* w2 = (const u16*)d_in[6];
    const u16* wrbf = (const u16*)d_in[7];
    u16* out = (u16*)d_out;
    const int N = in_sizes[1] / 3;
    const int E = in_sizes[2];

#define CAPV 64
    int cap = CAPV;
    size_t off_cnt, off_wr, off_phi, off_slots, off_ent, off_om, total;
    for (;;) {
        size_t o = 0;
        off_cnt = o; o = align_up(o + (size_t)(N + 1) * 4, 256);
        off_wr = o; o = align_up(o + 48 * 64 * 4, 256);
        off_phi = o; o = align_up(o + (size_t)N * 384 * 2, 256);
        off_slots = o; o = align_up(o + (size_t)N * cap * 16, 256);
        off_ent = o; o = align_up(o + (size_t)(E + 256) * 64, 256);
        off_om = o; o = align_up(o + (size_t)E * 16, 256);
        total = o;
        if (total <= ws_size || cap <= 16) break;
        cap >>= 1;
    }
    char* ws = (char*)d_ws;
    int* cnt = (int*)(ws + off_cnt);
    unsigned* wr = (unsigned*)(ws + off_wr);
    u16* phi = (u16*)(ws + off_phi);
    int4* slots = (int4*)(ws + off_slots);
    u32x4* entries = (u32x4*)(ws + off_ent);
    int4* ovf_meta = (int4*)(ws + off_om);

    const int tiles = (N + 31) / 32;
    const int front_blocks = (tiles + 3) / 4;
    (void)hipMemsetAsync(cnt, 0, align_up((size_t)(N + 1) * 4, 16), stream);
    k_front<<<front_blocks, 512, 0, stream>>>(sv, w1, w2, wrbf, phi, wr, pos, src, dst, freqs, cnt, slots, entries,
                                              ovf_meta, N, E, cap);
    int gblocks = (N + 3) / 4;
    if (gblocks > GBLOCKS) gblocks = GBLOCKS;
    k_gather<<<gblocks, 256, 0, stream>>>(sv, phi, cnt, slots, entries, (const u32x4*)wr, ovf_meta, out, N, cap);
}
```

```cpp
#include <hip/hip_runtime.h>
#include <hip/hip_bf16.h>
#include <stdint.h>

typedef __attribute__((ext_vector_type(8))) short bf16x8;
typedef __attribute__((ext_vector_type(16))) float f32x16;
typedef __attribute__((ext_vector_type(4))) unsigned u32x4;
typedef __attribute__((ext_vector_type(2))) __bf16 bf16x2_t;
typedef __attribute__((ext_vector_type(2))) float f32x2_t;
typedef unsigned short u16;

#define DI __device__ __forceinline__

DI float bf2f(u16 x) { return __uint_as_float(((unsigned)x) << 16); }
DI unsigned pack2(float a, float b) {
    f32x2_t v = {a, b};
    bf16x2_t r = __builtin_convertvector(v, bf16x2_t);
    return __builtin_bit_cast(unsigned, r);
}
DI float rb(float a) {
    return __uint_as_float(pack2(a, 0.f) << 16);
}
DI float lo16(unsigned u) { return __uint_as_float(u << 16); }
DI float hi16(unsigned u) { return __uint_as_float(u & 0xffff0000u); }

struct EdgeGeom {
    unsigned rbf[8];
    float fcut, rx, ry, rz;
};

struct __attribute__((aligned(4))) Pos8 { unsigned a, b; };
DI void load_pos(const u16* __restrict__ pos, int i, int last8, float& x, float& y, float& z) {
    int byte = 6 * i;
    int w = byte & ~3;
    w = w < last8 ? w : last8;
    const Pos8 v = *(const Pos8*)((const char*)pos + w);
    const unsigned long long q = (((unsigned long long)v.b << 32) | v.a) >> ((byte - w) * 8);
    x = __uint_as_float((unsigned)q << 16);
    y = __uint_as_float((unsigned)q & 0xffff0000u);
    z = __uint_as_float((unsigned)(q >> 32) << 16);
}
DI bool edge_dist(float sx, float sy, float sz, float tx, float ty, float tz, float& rx, float& ry, float& rz,
                  float& d) {
    rx = rb(sx - tx); ry = rb(sy - ty); rz = rb(sz - tz);
    float q = rb(rb(rx * rx) + rb(ry * ry) + rb(rz * rz));
    d = rb(sqrtf(rb(q + 1e-08f)));
    return d < 5.0f;
}
template <bool SLOW>
DI void edge_rest(const unsigned (&fq)[8], float rx, float ry, float rz, float d, EdgeGeom& g) {
    const float inv = 1.0f / d;
    g.rx = rb(rx * inv); g.ry = rb(ry * inv); g.rz = rb(rz * inv);
    const float ca = rb(rb(3.141592653589793f * d) / 5.0f);
    float c = rb(__builtin_amdgcn_cosf(ca * 0.15915494309189535f));
    g.fcut = rb(0.5f * rb(c + 1.0f));
#pragma unroll
    for (int kp = 0; kp < 8; ++kp) {
        float x0 = rb(d * lo16(fq[kp])), x1 = rb(d * hi16(fq[kp]));
        float s0, s1;
        if (SLOW) {
            s0 = sinf(x0); s1 = sinf(x1);
        } else {
            s0 = __builtin_amdgcn_sinf(x0 * 0.15915494309189535f);
            s1 = __builtin_amdgcn_sinf(x1 * 0.15915494309189535f);
        }
        g.rbf[kp] = pack2(rb(rb(s0) * inv), rb(rb(s1) * inv));
    }
}

template <int EU, bool SLOW>
DI void edge_loop(const u16* __restrict__ pos, const int* __restrict__ src, const int* __restrict__ dst,
                  const unsigned (&fq)[8], int* __restrict__ cnt, int2* __restrict__ slots,
                  u32x4* __restrict__ entries, int4* __restrict__ ovf_meta, u32x4* __restrict__ ovf_entries, int N,
                  int E, int cap, int ew, int nw, int lane) {
    const int nchunks = (E + 63) >> 6;
    const int last8 = (6 * N - 8) & ~3;
    for (int c0 = ew; c0 < nchunks; c0 += nw * EU) {
        int es[EU], ss[EU], ts[EU];
#pragma unroll
        for (int u = 0; u < EU; ++u) {
            int e = (c0 + u * nw) * 64 + lane;
            es[u] = e;
            int ec = e < E ? e : E - 1;
            ss[u] = src[ec]; ts[u] = dst[ec];
        }
        float rx[EU], ry[EU], rz[EU], d[EU];
        float px[EU], py[EU], pz[EU];
        bool act[EU];
#pragma unroll
        for (int u = 0; u < EU; ++u) {
            load_pos(pos, ss[u], last8, px[u], py[u], pz[u]);
            load_pos(pos, ts[u], last8, rx[u], ry[u], rz[u]);
        }
#pragma unroll
        for (int u = 0; u < EU; ++u)
            act[u] = edge_dist(px[u], py[u], pz[u], rx[u], ry[u], rz[u], rx[u], ry[u], rz[u], d[u]) && es[u] < E;
        int slot[EU];
#pragma unroll
        for (int u = 0; u < EU; ++u) slot[u] = act[u] ? atomicAdd(&cnt[ts[u]], 1) : 0;
#pragma unroll
        for (int u = 0; u < EU; ++u) {
            if (act[u]) {
                EdgeGeom g;
                edge_rest<SLOW>(fq, rx[u], ry[u], rz[u], d[u], g);
                u32x4* ep;
                if (slot[u] < cap) {
                    const size_t idx = (size_t)ts[u] * cap + slot[u];
                    slots[idx] = make_int2(es[u], ss[u]);
                    ep = entries + idx * 4;
                } else {
                    const int o = atomicAdd(&cnt[N], 1);
                    ovf_meta[o] = make_int4(es[u], ss[u], o, ts[u]);
                    ep = ovf_entries + (size_t)o * 4;
                }
                u32x4 a = {g.rbf[0], g.rbf[1], g.rbf[2], g.rbf[3]};
                u32x4 b = {g.rbf[4], g.rbf[5], g.rbf[6], g.rbf[7]};
                u32x4 c = {__float_as_uint(g.fcut), __float_as_uint(g.rx), __float_as_uint(g.ry),
                           __float_as_uint(g.rz)};
                ep[0] = a;
                ep[1] = b;
                ep[2] = c;
            }
        }
    }
}

typedef __attribute__((ext_vector_type(4))) short s16x4;
typedef __attribute__((address_space(3))) s16x4 lds_s16x4;

#define EUV 8
constexpr int W1_STRIDE = 256;
constexpr int W2_STRIDE = 768;
constexpr int W2_BASE = 128 * W1_STRIDE;
constexpr int STG_BASE = W2_BASE + 128 * W2_STRIDE;
constexpr int FRONT_LDS = STG_BASE + 4 * 32 * 144 + 16;

DI bf16x8 tr_frag(const char* lds, int off0, int off1) {
    s16x4 a = __builtin_amdgcn_ds_read_tr16_b64_v4i16((lds_s16x4*)(lds + off0));
    s16x4 b = __builtin_amdgcn_ds_read_tr16_b64_v4i16((lds_s16x4*)(lds + off1));
    return __builtin_shufflevector(a, b, 0, 1, 2, 3, 4, 5, 6, 7);
}

__global__ __launch_bounds__(512) void k_front(const u16* __restrict__ sv, const u16* __restrict__ w1,
                                               const u16* __restrict__ w2, const u16* __restrict__ wrbf,
                                               u16* __restrict__ phi, unsigned* __restrict__ wr,
                                               const u16* __restrict__ pos, const int* __restrict__ src,
                                               const int* __restrict__ dst, const u16* __restrict__ freqs,
                                               int* __restrict__ cnt, int2* __restrict__ slots,
                                               u32x4* __restrict__ entries, int4* __restrict__ ovf_meta,
                                               u32x4* __restrict__ ovf_entries, int N, int E, int cap,
                                               int gemm_blocks) {
    __shared__ __attribute__((aligned(16))) char lds[FRONT_LDS];
    const int tid = threadIdx.x;
    const int lane = tid & 63;
    const int wv = __builtin_amdgcn_readfirstlane(tid >> 6);
    const bool gemm_block = (int)blockIdx.x < gemm_blocks;
    int* bar = (int*)(lds + FRONT_LDS - 16);
    if (tid == 0) *bar = 0;
    __syncthreads();

    if (wv >= 4 || !gemm_block) {
        const int ew = gemm_block ? (int)blockIdx.x * 4 + (wv - 4)
                                  : gemm_blocks * 4 + ((int)blockIdx.x - gemm_blocks) * 8 + wv;
        const int nw = gemm_blocks * 4 + ((int)gridDim.x - gemm_blocks) * 8;
        unsigned fq[8];
#pragma unroll
        for (int kp = 0; kp < 8; ++kp) fq[kp] = (unsigned)freqs[2 * kp] | ((unsigned)freqs[2 * kp + 1] << 16);
        for (int i = ew * 64 + lane; i < 48 * 64; i += nw * 64) {
            int half = i & 1, ln = (i >> 1) & 63, gq = i >> 7;
            int g = gq >> 2, q = gq & 3;
            int ch = (g >> 1) * 128 + 2 * ln + (g & 1);
            int k = 4 * q + 2 * half;
            unsigned a = wrbf[k * 384 + ch], b = wrbf[(k + 1) * 384 + ch];
            wr[i] = a | (b << 16);
        }
        float fmax = 0.f;
#pragma unroll
        for (int kp = 0; kp < 8; ++kp) fmax = fmaxf(fmax, fmaxf(fabsf(lo16(fq[kp])), fabsf(hi16(fq[kp]))));
        if (fmax * 5.0f <= 60.0f)
            edge_loop<EUV, false>(pos, src, dst, fq, cnt, slots, entries, ovf_meta, ovf_entries, N, E, cap, ew, nw, lane);
        else
            edge_loop<1, true>(pos, src, dst, fq, cnt, slots, entries, ovf_meta, ovf_entries, N, E, cap, ew, nw, lane);
        return;
    }

    const int r = lane & 31, h = lane >> 5;
    const int tile = blockIdx.x * 4 + wv;
    const int node = tile * 32 + r;
    const bool valid = node < N;
    bf16x8 sf[8];
    {
        const bf16x8* srow = (const bf16x8*)(sv + (size_t)(valid ? node : 0) * 512);
#pragma unroll
        for (int ks = 0; ks < 8; ++ks) sf[ks] = srow[2 * ks + h];
    }
    {
        const u32x4* g1 = (const u32x4*)w1;
#pragma unroll
        for (int i = 0; i < 8; ++i) {
            int c = i * 256 + tid;
            int row = c >> 4, ch = c & 15;
            *(u32x4*)(lds + row * W1_STRIDE + ((ch * 16) ^ ((row & 3) << 6))) = g1[c];
        }
        const u32x4* g2 = (const u32x4*)w2;
#pragma unroll
        for (int i = 0; i < 24; ++i) {
            int c = i * 256 + tid;
            int row = c / 48, ch = c % 48;
            *(u32x4*)(lds + W2_BASE + row * W2_STRIDE + ((ch * 16) ^ ((row & 3) << 6))) = g2[c];
        }
    }
    __builtin_amdgcn_fence(__ATOMIC_RELEASE, "workgroup");
    if (lane == 0) __hip_atomic_fetch_add(bar, 1, __ATOMIC_RELAXED, __HIP_MEMORY_SCOPE_WORKGROUP);
    while (__hip_atomic_load(bar, __ATOMIC_RELAXED, __HIP_MEMORY_SCOPE_WORKGROUP) < 4) __builtin_amdgcn_s_sleep(1);
    __builtin_amdgcn_fence(__ATOMIC_ACQUIRE, "workgroup");

    const int i16 = lane & 15, q = i16 >> 2, p4 = i16 & 3, g1b = (lane >> 4) & 1;
    const int colb = ((16 * g1b + 4 * p4) * 2) ^ (q << 6);
    bf16x8 hf[8];
#pragma unroll
    for (int t = 0; t < 4; ++t) {
        bf16x8 fr[8];
#pragma unroll
        for (int ks = 0; ks < 8; ++ks) {
            int off = (16 * ks + 8 * h + q) * W1_STRIDE + ((64 * t) ^ colb);
            fr[ks] = tr_frag(lds, off, off + 4 * W1_STRIDE);
        }
        f32x16 acc;
#pragma unroll
        for (int i = 0; i < 16; ++i) acc[i] = 0.f;
#pragma unroll
        for (int ks = 0; ks < 8; ++ks) acc = __builtin_amdgcn_mfma_f32_32x32x16_bf16(fr[ks], sf[ks], acc, 0, 0, 0);
        unsigned p[8];
#pragma unroll
        for (int i = 0; i < 8; ++i) {
            float x0 = rb(acc[2 * i]), x1 = rb(acc[2 * i + 1]);
            float y0 = x0 * __builtin_amdgcn_rcpf(1.0f + __expf(-x0)), y1 = x1 * __builtin_amdgcn_rcpf(1.0f + __expf(-x1));
            p[i] = pack2(y0, y1);
        }
        u32x4 a = {p[0], p[1], p[2], p[3]}, b = {p[4], p[5], p[6], p[7]};
        hf[2 * t] = __builtin_bit_cast(bf16x8, a);
        hf[2 * t + 1] = __builtin_bit_cast(bf16x8, b);
    }
    char* stg = lds + STG_BASE + wv * (32 * 144);
    const int rrow = lane >> 3, rch = lane & 7;
    bf16x8 fa[8], fb[8];
#pragma unroll
    for (int ks = 0; ks < 8; ++ks) {
        int off = W2_BASE + (16 * ks + 4 * h + q) * W2_STRIDE + colb;
        fa[ks] = tr_frag(lds, off, off + 8 * W2_STRIDE);
    }
#pragma unroll
    for (int c2 = 0; c2 < 6; ++c2) {
        {
            const int c = 2 * c2 + 1;
#pragma unroll
            for (int ks = 0; ks < 8; ++ks) {
                int off = W2_BASE + (16 * ks + 4 * h + q) * W2_STRIDE + (c >> 2) * 256 + (((c & 3) * 64) ^ colb);
                fb[ks] = tr_frag(lds, off, off + 8 * W2_STRIDE);
            }
        }
        {
            f32x16 acc;
#pragma unroll
            for (int i = 0; i < 16; ++i) acc[i] = 0.f;
#pragma unroll
            for (int ks = 0; ks < 8; ++ks) acc = __builtin_amdgcn_mfma_f32_32x32x16_bf16(fa[ks], hf[ks], acc, 0, 0, 0);
#pragma unroll
            for (int g = 0; g < 4; ++g) {
                uint2 o;
                o.x = pack2(acc[4 * g], acc[4 * g + 1]);
                o.y = pack2(acc[4 * g + 2], acc[4 * g + 3]);
                *(uint2*)(stg + r * 144 + 16 * g + 8 * h) = o;
            }
        }
        if (c2 < 5) {
            const int c = 2 * c2 + 2;
#pragma unroll
            for (int ks = 0; ks < 8; ++ks) {
                int off = W2_BASE + (16 * ks + 4 * h + q) * W2_STRIDE + (c >> 2) * 256 + (((c & 3) * 64) ^ colb);
                fa[ks] = tr_frag(lds, off, off + 8 * W2_STRIDE);
            }
        }
        {
            f32x16 acc;
#pragma unroll
            for (int i = 0; i < 16; ++i) acc[i] = 0.f;
#pragma unroll
            for (int ks = 0; ks < 8; ++ks) acc = __builtin_amdgcn_mfma_f32_32x32x16_bf16(fb[ks], hf[ks], acc, 0, 0, 0);
#pragma unroll
            for (int g = 0; g < 4; ++g) {
                uint2 o;
                o.x = pack2(acc[4 * g], acc[4 * g + 1]);
                o.y = pack2(acc[4 * g + 2], acc[4 * g + 3]);
                *(uint2*)(stg + r * 144 + 64 + 16 * g + 8 * h) = o;
            }
        }
        __builtin_amdgcn_wave_barrier();
        u32x4 v[4];
#pragma unroll
        for (int i = 0; i < 4; ++i) v[i] = *(const u32x4*)(stg + (rrow + 8 * i) * 144 + rch * 16);
#pragma unroll
        for (int i = 0; i < 4; ++i) {
            int nd = tile * 32 + rrow + 8 * i;
            if (nd < N) *(u32x4*)(phi + (size_t)nd * 384 + c2 * 64 + rch * 8) = v[i];
        }
        __builtin_amdgcn_wave_barrier();
    }
}

struct RowData { unsigned ps, pvv, pvr, vx, vy, vz; };

typedef __attribute__((address_space(1))) char gchar;
typedef __attribute__((address_space(1))) unsigned gu32;
DI gchar* uptr(const void* p) {
    const unsigned long long v = (unsigned long long)p;
    const unsigned lo = __builtin_amdgcn_readfirstlane((unsigned)v);
    const unsigned hi = __builtin_amdgcn_readfirstlane((unsigned)(v >> 32));
    return (gchar*)(((unsigned long long)hi << 32) | lo);
}

DI void load_rows(const u16* __restrict__ sv, const u16* __restrict__ phi, int s, int lane, RowData& r) {
    const unsigned lo4 = (unsigned)lane * 4u;
    const gchar* p = uptr(phi + (size_t)s * 384);
    const gchar* v = uptr(sv + (size_t)s * 512);
    r.ps = *(const gu32*)(p + lo4);
    r.pvv = *(const gu32*)(p + lo4 + 256);
    r.pvr = *(const gu32*)(p + lo4 + 512);
    r.vx = *(const gu32*)(v + lo4 + 256);
    r.vy = *(const gu32*)(v + lo4 + 512);
    r.vz = *(const gu32*)(v + lo4 + 768);
}

typedef __attribute__((ext_vector_type(4))) float f32x4;
typedef __attribute__((ext_vector_type(2))) unsigned u32x2;

DI void accum_edge(const RowData& r, const float (&wf)[6], float hx, float hy, float hz, float (&acc)[8]) {
    acc[0] += lo16(r.ps) * wf[0];
    acc[1] += hi16(r.ps) * wf[1];
    float mvv0 = lo16(r.pvv) * wf[2], mvv1 = hi16(r.pvv) * wf[3];
    float mvr0 = lo16(r.pvr) * wf[4], mvr1 = hi16(r.pvr) * wf[5];
    acc[2] += mvv0 * lo16(r.vx) + mvr0 * hx;
    acc[3] += mvv1 * hi16(r.vx) + mvr1 * hx;
    acc[4] += mvv0 * lo16(r.vy) + mvr0 * hy;
    acc[5] += mvv1 * hi16(r.vy) + mvr1 * hy;
    acc[6] += mvv0 * lo16(r.vz) + mvr0 * hz;
    acc[7] += mvv1 * hi16(r.vz) + mvr1 * hz;
}

DI void wf_mfma(const u32x4& a0, const u32x4& a1, const u32x2* __restrict__ wl, int lane, unsigned (&P)[3][4]) {
    const u32x2 aq[4] = {{a0[0], a0[1]}, {a0[2], a0[3]}, {a1[0], a1[1]}, {a1[2], a1[3]}};
#pragma unroll
    for (int seg = 0; seg < 3; ++seg) {
        f32x4 d0 = {0.f, 0.f, 0.f, 0.f}, d1 = {0.f, 0.f, 0.f, 0.f};
#pragma unroll
        for (int q = 0; q < 4; ++q) {
            d0 = __builtin_amdgcn_mfma_f32_4x4x4bf16_1k(__builtin_bit_cast(s16x4, aq[q]),
                                                       __builtin_bit_cast(s16x4, wl[((2 * seg) * 4 + q) * 64 + lane]),
                                                       d0, 0, 0, 0);
            d1 = __builtin_amdgcn_mfma_f32_4x4x4bf16_1k(
                __builtin_bit_cast(s16x4, aq[q]), __builtin_bit_cast(s16x4, wl[((2 * seg + 1) * 4 + q) * 64 + lane]),
                d1, 0, 0, 0);
        }
#pragma unroll
        for (int u = 0; u < 4; ++u) P[seg][u] = pack2(d0[u], d1[u]);
        __builtin_amdgcn_sched_barrier(0);
    }
}

struct Chunk {
    u32x4 a0, a1, a2;
    RowData rd[4];
};

DI void chunk_load(const u16* __restrict__ sv, const u16* __restrict__ phi, const u32x4* __restrict__ ent_node,
                   int lane, int c, int base, int rank, int sly, Chunk& ck) {
    int idx[4], sr[4];
#pragma unroll
    for (int u = 0; u < 4; ++u) {
        const int rr = base + u < c ? base + u : base;
        idx[u] = __builtin_ctzll(__ballot(lane < c && rank == rr) | (1ull << 63));
        sr[u] = __builtin_amdgcn_readlane(sly, idx[u]);
    }
    const int u4 = lane & 3;
    int myidx = idx[0];
    myidx = u4 == 1 ? idx[1] : myidx;
    myidx = u4 == 2 ? idx[2] : myidx;
    myidx = u4 == 3 ? idx[3] : myidx;
    const u32x4* ep = ent_node + myidx * 4;
    ck.a0 = ep[0]; ck.a1 = ep[1]; ck.a2 = ep[2];
#pragma unroll
    for (int u = 0; u < 4; ++u) load_rows(sv, phi, sr[u], lane, ck.rd[u]);
}

DI void chunk_compute(const Chunk& ck, const u32x2* __restrict__ wl, int lane, int c, int base, float (&acc)[8]) {
    unsigned P[3][4];
    int lofs = lane;
    asm volatile("" : "+v"(lofs));
    wf_mfma(ck.a0, ck.a1, wl, lofs, P);
#pragma unroll
    for (int u = 0; u < 4; ++u) {
        if (u == 0 || base + u < c) {
            const float fc = __uint_as_float(__builtin_amdgcn_readlane(ck.a2[0], u));
            const float hx = __uint_as_float(__builtin_amdgcn_readlane(ck.a2[1], u));
            const float hy = __uint_as_float(__builtin_amdgcn_readlane(ck.a2[2], u));
            const float hz = __uint_as_float(__builtin_amdgcn_readlane(ck.a2[3], u));
            float wf[6];
#pragma unroll
            for (int sg = 0; sg < 3; ++sg) {
                wf[2 * sg] = lo16(P[sg][u]) * fc;
                wf[2 * sg + 1] = hi16(P[sg][u]) * fc;
            }
            accum_edge(ck.rd[u], wf, hx, hy, hz, acc);
        }
    }
}

#define GOCC 5
__global__ __launch_bounds__(256, GOCC) void k_gather(const u16* __restrict__ sv, const u16* __restrict__ phi,
                                                const int* __restrict__ cnt, const int2* __restrict__ slots,
                                                const u32x4* __restrict__ entries, const u32x4* __restrict__ wr,
                                                const int4* __restrict__ ovf_meta,
                                                const u32x4* __restrict__ ovf_entries,
                                                u16* __restrict__ out, int N, int cap) {
    __shared__ __attribute__((aligned(16))) u32x4 wlds[768];
    const int lane = threadIdx.x & 63;
    const int wave = __builtin_amdgcn_readfirstlane(blockIdx.x * 4 + (threadIdx.x >> 6));
    const int nw = gridDim.x * 4;
#pragma unroll
    for (int i = 0; i < 3; ++i) wlds[i * 256 + threadIdx.x] = wr[i * 256 + threadIdx.x];
    const u32x2* wl = (const u32x2*)wlds;

    int node = wave;
    const int n0 = node < N ? node : N - 1;
    int c_next = cnt[n0];
    int2 sl_next = slots[(size_t)n0 * cap + (lane & 15)];
    __syncthreads();
    bool any_big = false;
    for (; node < N; node += nw) {
        const int c = __builtin_amdgcn_readfirstlane(c_next);
        const int2 sl = sl_next;
        const int nn = node + nw < N ? node + nw : N - 1;
        const gu32* own = (const gu32*)(uptr(sv + (size_t)node * 512) + (unsigned)lane * 4u);
        gu32* op = (gu32*)(uptr(out + (size_t)node * 512) + (unsigned)lane * 4u);
        if (c == 0 || c > 16) {
            if (c == 0) {
                const unsigned o0 = own[0], o1 = own[64], o2 = own[128], o3 = own[192];
                c_next = cnt[nn];
                sl_next = slots[(size_t)nn * cap + (lane & 15)];
                op[0] = o0; op[64] = o1; op[128] = o2; op[192] = o3;
            } else {
                any_big = true;
                c_next = cnt[nn];
                sl_next = slots[(size_t)nn * cap + (lane & 15)];
            }
            continue;
        }
        float acc[8];
#pragma unroll
        for (int i = 0; i < 8; ++i) acc[i] = 0.f;
        int rank = 0;
        for (int j = 0; j < c; ++j) {
            int ej = __builtin_amdgcn_readlane(sl.x, j);
            rank += (ej < sl.x) ? 1 : 0;
        }
        const u32x4* ent_node = entries + (size_t)node * cap * 4;
        unsigned o0, o1, o2, o3;
        {
            Chunk ck;
            chunk_load(sv, phi, ent_node, lane, c, 0, rank, sl.y, ck);
            o0 = own[0]; o1 = own[64]; o2 = own[128]; o3 = own[192];
            c_next = cnt[nn];
            sl_next = slots[(size_t)nn * cap + (lane & 15)];
            chunk_compute(ck, wl, lane, c, 0, acc);
        }
        for (int base = 4; base < c; base += 4) {
            Chunk ck;
            chunk_load(sv, phi, ent_node, lane, c, base, rank, sl.y, ck);
            chunk_compute(ck, wl, lane, c, base, acc);
        }
        op[0] = pack2(lo16(o0) + acc[0], hi16(o0) + acc[1]);
        op[64] = pack2(lo16(o1) + acc[2], hi16(o1) + acc[3]);
        op[128] = pack2(lo16(o2) + acc[4], hi16(o2) + acc[5]);
        op[192] = pack2(lo16(o3) + acc[6], hi16(o3) + acc[7]);
    }
    if (!any_big) return;

    for (node = wave; node < N; node += nw) {
        const int c = __builtin_amdgcn_readfirstlane(cnt[node]);
        if (c <= 16) continue;
        int2 sl = make_int2(0, 0);
        if (lane < cap) sl = slots[(size_t)node * cap + lane];
        const unsigned* own = (const unsigned*)(sv + (size_t)node * 512) + lane;
        unsigned o0 = own[0], o1 = own[64], o2 = own[128], o3 = own[192];
        float acc[8];
#pragma unroll
        for (int i = 0; i < 8; ++i) acc[i] = 0.f;
        const int L = (c > cap) ? cnt[N] : 0;
        const int nb = c < cap ? c : cap;
        int last = -1;
        for (int it = 0; it < c; ++it) {
            int best = 0x7fffffff, loc = 0, bs = 0;
            if (lane < nb && sl.x > last) { best = sl.x; loc = lane; bs = sl.y; }
            for (int i = lane; i < L; i += 64) {
                int4 o = ovf_meta[i];
                if (o.w == node && o.x > last && o.x < best) { best = o.x; loc = -1 - i; bs = o.y; }
            }
            int mn = best;
#pragma unroll
            for (int sft = 32; sft >= 1; sft >>= 1) {
                int other = __shfl_xor(mn, sft, 64);
                mn = other < mn ? other : mn;
            }
            unsigned long long m = __ballot(best == mn);
            int owner = __builtin_ctzll(m);
            int l2 = __builtin_amdgcn_readlane(loc, owner);
            int s = __builtin_amdgcn_readlane(bs, owner);
            const u32x4* ep = (l2 >= 0) ? entries + ((size_t)node * cap + l2) * 4
                                        : ovf_entries + (size_t)(-1 - l2) * 4;
            RowData rd;
            load_rows(sv, phi, s, lane, rd);
            u32x4 a0 = ep[0], a1 = ep[1], a2 = ep[2];
            unsigned P[3][4];
            int lofs = lane;
            asm volatile("" : "+v"(lofs));
            wf_mfma(a0, a1, wl, lofs, P);
            float wf[6];
#pragma unroll
            for (int sg = 0; sg < 3; ++sg) {
                wf[2 * sg] = lo16(P[sg][0]) * __uint_as_float(a2[0]);
                wf[2 * sg + 1] = hi16(P[sg][0]) * __uint_as_float(a2[0]);
            }
            accum_edge(rd, wf, __uint_as_float(a2[1]), __uint_as_float(a2[2]), __uint_as_float(a2[3]), acc);
            last = mn;
        }
        unsigned* op = (unsigned*)(out + (size_t)node * 512) + lane;
        op[0] = pack2(lo16(o0) + acc[0], hi16(o0) + acc[1]);
        op[64] = pack2(lo16(o1) + acc[2], hi16(o1) + acc[3]);
        op[128] = pack2(lo16(o2) + acc[4], hi16(o2) + acc[5]);
        op[192] = pack2(lo16(o3) + acc[6], hi16(o3) + acc[7]);
    }
}

static inline size_t align_up(size_t x, size_t a) { return (x + a - 1) / a * a; }

#define GBLOCKS 1280

extern "C" void kernel_launch(void* const* d_in, const int* in_sizes, int n_in, void* d_out, int out_size,
                              void* d_ws, size_t ws_size, hipStream_t stream) {
    const u16* sv = (const u16*)d_in[0];
    const u16* pos = (const u16*)d_in[1];
    const int* src = (const int*)d_in[2];
    const int* dst = (const int*)d_in[3];
    const u16* freqs = (const u16*)d_in[4];
    const u16* w1 = (const u16*)d_in[5];
    const u16* w2 = (const u16*)d_in[6];
    const u16* wrbf = (const u16*)d_in[7];
    u16* out = (u16*)d_out;
    const int N = in_sizes[1] / 3;
    const int E = in_sizes[2];

#define CAPV 32
    int cap = CAPV;
    size_t off_cnt, off_wr, off_phi, off_slots, off_ent, off_om, off_oe, total;
    for (;;) {
        size_t o = 0;
        off_cnt = o; o = align_up(o + (size_t)(N + 1) * 4, 256);
        off_wr = o; o = align_up(o + 48 * 64 * 4, 256);
        off_phi = o; o = align_up(o + (size_t)N * 384 * 2, 256);
        off_slots = o; o = align_up(o + (size_t)N * cap * 8, 256);
        off_ent = o; o = align_up(o + (size_t)N * cap * 64, 256);
        off_om = o; o = align_up(o + (size_t)E * 16, 256);
        off_oe = o; o = align_up(o + (size_t)E * 64, 256);
        total = o;
        if (total <= ws_size || cap <= 16) break;
        cap >>= 1;
    }
    char* ws = (char*)d_ws;
    int* cnt = (int*)(ws + off_cnt);
    unsigned* wr = (unsigned*)(ws + off_wr);
    u16* phi = (u16*)(ws + off_phi);
    int2* slots = (int2*)(ws + off_slots);
    u32x4* entries = (u32x4*)(ws + off_ent);
    int4* ovf_meta = (int4*)(ws + off_om);
    u32x4* ovf_entries = (u32x4*)(ws + off_oe);

    const int tiles = (N + 31) / 32;
    const int front_blocks = (tiles + 3) / 4;
    (void)hipMemsetAsync(cnt, 0, align_up((size_t)(N + 1) * 4, 16), stream);
    const int fgrid = front_blocks < 256 ? 256 : front_blocks;
    k_front<<<fgrid, 512, 0, stream>>>(sv, w1, w2, wrbf, phi, wr, pos, src, dst, freqs, cnt, slots, entries, ovf_meta,
                                       ovf_entries, N, E, cap, front_blocks);
    int gblocks = (N + 3) / 4;
    if (gblocks > GBLOCKS) gblocks = GBLOCKS;
    k_gather<<<gblocks, 256, 0, stream>>>(sv, phi, cnt, slots, entries, (const u32x4*)wr, ovf_meta, ovf_entries, out,
                                          N, cap);
}
```

```cpp
#include <hip/hip_runtime.h>
#include <hip/hip_bf16.h>
#include <stdint.h>

typedef __attribute__((ext_vector_type(8))) short bf16x8;
typedef __attribute__((ext_vector_type(16))) float f32x16;
typedef __attribute__((ext_vector_type(4))) unsigned u32x4;
typedef __attribute__((ext_vector_type(2))) __bf16 bf16x2_t;
typedef __attribute__((ext_vector_type(2))) float f32x2_t;
typedef unsigned short u16;

#define DI __device__ __forceinline__

DI float bf2f(u16 x) { return __uint_as_float(((unsigned)x) << 16); }
DI unsigned pack2(float a, float b) {
    f32x2_t v = {a, b};
    bf16x2_t r = __builtin_convertvector(v, bf16x2_t);
    return __builtin_bit_cast(unsigned, r);
}
DI float rb(float a) {
    return __uint_as_float(pack2(a, 0.f) << 16);
}
DI float lo16(unsigned u) { return __uint_as_float(u << 16); }
DI float hi16(unsigned u) { return __uint_as_float(u & 0xffff0000u); }

struct EdgeGeom {
    unsigned rbf[8];
    float fcut, rx, ry, rz;
};

struct __attribute__((aligned(4))) Pos8 { unsigned a, b; };
DI void load_pos(const u16* __restrict__ pos, int i, int last8, float& x, float& y, float& z) {
    const int byte = 6 * i;
    const int w = byte & ~3;
    if (w <= last8) {
        const Pos8 v = *(const Pos8*)((const char*)pos + w);
        const unsigned long long q = (((unsigned long long)v.b << 32) | v.a) >> ((byte - w) * 8);
        x = __uint_as_float((unsigned)q << 16);
        y = __uint_as_float((unsigned)q & 0xffff0000u);
        z = __uint_as_float((unsigned)(q >> 32) << 16);
    } else {
        x = bf2f(pos[3 * i]); y = bf2f(pos[3 * i + 1]); z = bf2f(pos[3 * i + 2]);
    }
}
DI bool edge_dist(float sx, float sy, float sz, float tx, float ty, float tz, float& rx, float& ry, float& rz,
                  float& d) {
    rx = rb(sx - tx); ry = rb(sy - ty); rz = rb(sz - tz);
    float q = rb(rb(rx * rx) + rb(ry * ry) + rb(rz * rz));
    d = rb(sqrtf(rb(q + 1e-08f)));
    return d < 5.0f;
}
template <bool SLOW>
DI void edge_rest(const unsigned (&fq)[8], float rx, float ry, float rz, float d, EdgeGeom& g) {
    const float inv = 1.0f / d;
    g.rx = rb(rx * inv); g.ry = rb(ry * inv); g.rz = rb(rz * inv);
    const float ca = rb(rb(3.141592653589793f * d) / 5.0f);
    float c = rb(__builtin_amdgcn_cosf(ca * 0.15915494309189535f));
    g.fcut = rb(0.5f * rb(c + 1.0f));
#pragma unroll
    for (int kp = 0; kp < 8; ++kp) {
        float x0 = rb(d * lo16(fq[kp])), x1 = rb(d * hi16(fq[kp]));
        float s0, s1;
        if (SLOW) {
            s0 = sinf(x0); s1 = sinf(x1);
        } else {
            s0 = __builtin_amdgcn_sinf(x0 * 0.15915494309189535f);
            s1 = __builtin_amdgcn_sinf(x1 * 0.15915494309189535f);
        }
        g.rbf[kp] = pack2(rb(rb(s0) * inv), rb(rb(s1) * inv));
    }
}

template <int EU, bool SLOW>
DI void edge_loop(const u16* __restrict__ pos, const int* __restrict__ src, const int* __restrict__ dst,
                  const unsigned (&fq)[8], int* __restrict__ cnt, int2* __restrict__ slots,
                  u32x4* __restrict__ entries, int4* __restrict__ ovf_meta, u32x4* __restrict__ ovf_entries, int N,
                  int E, int cap, int ew, int nw, int lane) {
    const int nchunks = (E + 63) >> 6;
    const int last8 = 6 * N - 8;
    for (int c0 = ew; c0 < nchunks; c0 += nw * EU) {
        int es[EU], ss[EU], ts[EU];
#pragma unroll
        for (int u = 0; u < EU; ++u) {
            int e = (c0 + u * nw) * 64 + lane;
            es[u] = e;
            int ec = e < E ? e : E - 1;
            ss[u] = src[ec]; ts[u] = dst[ec];
        }
        float rx[EU], ry[EU], rz[EU], d[EU];
        float px[EU], py[EU], pz[EU];
        bool act[EU];
#pragma unroll
        for (int u = 0; u < EU; ++u) {
            load_pos(pos, ss[u], last8, px[u], py[u], pz[u]);
            load_pos(pos, ts[u], last8, rx[u], ry[u], rz[u]);
        }
#pragma unroll
        for (int u = 0; u < EU; ++u)
            act[u] = edge_dist(px[u], py[u], pz[u], rx[u], ry[u], rz[u], rx[u], ry[u], rz[u], d[u]) && es[u] < E;
        int slot[EU];
#pragma unroll
        for (int u = 0; u < EU; ++u) slot[u] = act[u] ? atomicAdd(&cnt[ts[u]], 1) : 0;
#pragma unroll
        for (int u = 0; u < EU; ++u) {
            if (act[u]) {
                EdgeGeom g;
                edge_rest<SLOW>(fq, rx[u], ry[u], rz[u], d[u], g);
                u32x4* ep;
                if (slot[u] < cap) {
                    const size_t idx = (size_t)ts[u] * cap + slot[u];
                    slots[idx] = make_int2(es[u], ss[u]);
                    ep = entries + idx * 4;
                } else {
                    const int o = atomicAdd(&cnt[N], 1);
                    ovf_meta[o] = make_int4(es[u], ss[u], o, ts[u]);
                    ep = ovf_entries + (size_t)o * 4;
                }
                u32x4 a = {g.rbf[0], g.rbf[1], g.rbf[2], g.rbf[3]};
                u32x4 b = {g.rbf[4], g.rbf[5], g.rbf[6], g.rbf[7]};
                u32x4 c = {__float_as_uint(g.fcut), __float_as_uint(g.rx), __float_as_uint(g.ry),
                           __float_as_uint(g.rz)};
                ep[0] = a;
                ep[1] = b;
                ep[2] = c;
            }
        }
    }
}

typedef __attribute__((ext_vector_type(4))) short s16x4;
typedef __attribute__((address_space(3))) s16x4 lds_s16x4;

#define EUV 8
constexpr int W1_STRIDE = 256;
constexpr int W2_STRIDE = 768;
constexpr int W2_BASE = 128 * W1_STRIDE;
constexpr int STG_BASE = W2_BASE + 128 * W2_STRIDE;
constexpr int FRONT_LDS = STG_BASE + 4 * 32 * 144 + 16;

DI bf16x8 tr_frag(const char* lds, int off0, int off1) {
    s16x4 a = __builtin_amdgcn_ds_read_tr16_b64_v4i16((lds_s16x4*)(lds + off0));
    s16x4 b = __builtin_amdgcn_ds_read_tr16_b64_v4i16((lds_s16x4*)(lds + off1));
    return __builtin_shufflevector(a, b, 0, 1, 2, 3, 4, 5, 6, 7);
}

#define PFW 4
__global__ __launch_bounds__(512 + 64 * PFW) void k_front(const u16* __restrict__ sv, const u16* __restrict__ w1,
                                               const u16* __restrict__ w2, const u16* __restrict__ wrbf,
                                               u16* __restrict__ phi, unsigned* __restrict__ wr,
                                               const u16* __restrict__ pos, const int* __restrict__ src,
                                               const int* __restrict__ dst, const u16* __restrict__ freqs,
                                               int* __restrict__ cnt, int2* __restrict__ slots,
                                               u32x4* __restrict__ entries, int4* __restrict__ ovf_meta,
                                               u32x4* __restrict__ ovf_entries, int N, int E, int cap,
                                               int gemm_blocks) {
    __shared__ __attribute__((aligned(16))) char lds[FRONT_LDS];
    const int tid = threadIdx.x;
    const int lane = tid & 63;
    const int wv = __builtin_amdgcn_readfirstlane(tid >> 6);
    const bool gemm_block = (int)blockIdx.x < gemm_blocks;
    int* bar = (int*)(lds + FRONT_LDS - 16);
    if (tid == 0) *bar = 0;
    __syncthreads();

    if (wv >= 8) {
        const int pw = blockIdx.x * PFW + (wv - 8), npw = gridDim.x * PFW;
        unsigned x = 0;
        for (int n = pw; n < N; n += npw * 4) {
            u32x4 a[4];
#pragma unroll
            for (int u = 0; u < 4; ++u) {
                const int nn = n + u * npw < N ? n + u * npw : n;
                a[u] = lane < 48 ? *((const u32x4*)(sv + (size_t)nn * 512 + 128) + lane) : u32x4{0, 0, 0, 0};
            }
#pragma unroll
            for (int u = 0; u < 4; ++u) x ^= a[u][0] ^ a[u][1] ^ a[u][2] ^ a[u][3];
        }
        asm volatile("" ::"v"(x));
        return;
    }
    if (wv >= 4 || !gemm_block) {
        const int ew = gemm_block ? (int)blockIdx.x * 4 + (wv - 4)
                                  : gemm_blocks * 4 + ((int)blockIdx.x - gemm_blocks) * 8 + wv;
        const int nw = gemm_blocks * 4 + ((int)gridDim.x - gemm_blocks) * 8;
        unsigned fq[8];
#pragma unroll
        for (int kp = 0; kp < 8; ++kp) fq[kp] = (unsigned)freqs[2 * kp] | ((unsigned)freqs[2 * kp + 1] << 16);
        for (int i = ew * 64 + lane; i < 48 * 64; i += nw * 64) {
            int half = i & 1, ln = (i >> 1) & 63, gq = i >> 7;
            int g = gq >> 2, q = gq & 3;
            int ch = (g >> 1) * 128 + 2 * ln + (g & 1);
            int k = 4 * q + 2 * half;
            unsigned a = wrbf[k * 384 + ch], b = wrbf[(k + 1) * 384 + ch];
            wr[i] = a | (b << 16);
        }
        float fmax = 0.f;
#pragma unroll
        for (int kp = 0; kp < 8; ++kp) fmax = fmaxf(fmax, fmaxf(fabsf(lo16(fq[kp])), fabsf(hi16(fq[kp]))));
        if (fmax * 5.0f <= 60.0f)
            edge_loop<EUV, false>(pos, src, dst, fq, cnt, slots, entries, ovf_meta, ovf_entries, N, E, cap, ew, nw, lane);
        else
            edge_loop<1, true>(pos, src, dst, fq, cnt, slots, entries, ovf_meta, ovf_entries, N, E, cap, ew, nw, lane);
        return;
    }

    const int r = lane & 31, h = lane >> 5;
    const int tile = blockIdx.x * 4 + wv;
    const int node = tile * 32 + r;
    const bool valid = node < N;
    bf16x8 sf[8];
    {
        const bf16x8* srow = (const bf16x8*)(sv + (size_t)(valid ? node : 0) * 512);
#pragma unroll
        for (int ks = 0; ks < 8; ++ks) sf[ks] = srow[2 * ks + h];
    }
    {
        const u32x4* g1 = (const u32x4*)w1;
#pragma unroll
        for (int i = 0; i < 8; ++i) {
            int c = i * 256 + tid;
            int row = c >> 4, ch = c & 15;
            *(u32x4*)(lds + row * W1_STRIDE + ((ch * 16) ^ ((row & 3) << 6))) = g1[c];
        }
        const u32x4* g2 = (const u32x4*)w2;
#pragma unroll
        for (int i = 0; i < 24; ++i) {
            int c = i * 256 + tid;
            int row = c / 48, ch = c % 48;
            *(u32x4*)(lds + W2_BASE + row * W2_STRIDE + ((ch * 16) ^ ((row & 3) << 6))) = g2[c];
        }
    }
    __builtin_amdgcn_fence(__ATOMIC_RELEASE, "workgroup");
    if (lane == 0) __hip_atomic_fetch_add(bar, 1, __ATOMIC_RELAXED, __HIP_MEMORY_SCOPE_WORKGROUP);
    while (__hip_atomic_load(bar, __ATOMIC_RELAXED, __HIP_MEMORY_SCOPE_WORKGROUP) < 4) __builtin_amdgcn_s_sleep(1);
    __builtin_amdgcn_fence(__ATOMIC_ACQUIRE, "workgroup");

    const int i16 = lane & 15, q = i16 >> 2, p4 = i16 & 3, g1b = (lane >> 4) & 1;
    const int colb = ((16 * g1b + 4 * p4) * 2) ^ (q << 6);
    bf16x8 hf[8];
#pragma unroll
    for (int t = 0; t < 4; ++t) {
        bf16x8 fr[8];
#pragma unroll
        for (int ks = 0; ks < 8; ++ks) {
            int off = (16 * ks + 8 * h + q) * W1_STRIDE + ((64 * t) ^ colb);
            fr[ks] = tr_frag(lds, off, off + 4 * W1_STRIDE);
        }
        f32x16 acc;
#pragma unroll
        for (int i = 0; i < 16; ++i) acc[i] = 0.f;
#pragma unroll
        for (int ks = 0; ks < 8; ++ks) acc = __builtin_amdgcn_mfma_f32_32x32x16_bf16(fr[ks], sf[ks], acc, 0, 0, 0);
        unsigned p[8];
#pragma unroll
        for (int i = 0; i < 8; ++i) {
            float x0 = rb(acc[2 * i]), x1 = rb(acc[2 * i + 1]);
            float y0 = x0 * __builtin_amdgcn_rcpf(1.0f + __expf(-x0)), y1 = x1 * __builtin_amdgcn_rcpf(1.0f + __expf(-x1));
            p[i] = pack2(y0, y1);
        }
        u32x4 a = {p[0], p[1], p[2], p[3]}, b = {p[4], p[5], p[6], p[7]};
        hf[2 * t] = __builtin_bit_cast(bf16x8, a);
        hf[2 * t + 1] = __builtin_bit_cast(bf16x8, b);
    }
    char* stg = lds + STG_BASE + wv * (32 * 144);
    const int rrow = lane >> 3, rch = lane & 7;
    const __amdgpu_buffer_rsrc_t rphi = __builtin_amdgcn_make_buffer_rsrc((void*)phi, 0, N * 768, 0x00020000);
    bf16x8 fa[8], fb[8];
#pragma unroll
    for (int ks = 0; ks < 8; ++ks) {
        int off = W2_BASE + (16 * ks + 4 * h + q) * W2_STRIDE + colb;
        fa[ks] = tr_frag(lds, off, off + 8 * W2_STRIDE);
    }
#pragma unroll
    for (int c2 = 0; c2 < 6; ++c2) {
        {
            const int c = 2 * c2 + 1;
#pragma unroll
            for (int ks = 0; ks < 8; ++ks) {
                int off = W2_BASE + (16 * ks + 4 * h + q) * W2_STRIDE + (c >> 2) * 256 + (((c & 3) * 64) ^ colb);
                fb[ks] = tr_frag(lds, off, off + 8 * W2_STRIDE);
            }
        }
        {
            f32x16 acc;
#pragma unroll
            for (int i = 0; i < 16; ++i) acc[i] = 0.f;
#pragma unroll
            for (int ks = 0; ks < 8; ++ks) acc = __builtin_amdgcn_mfma_f32_32x32x16_bf16(fa[ks], hf[ks], acc, 0, 0, 0);
#pragma unroll
            for (int g = 0; g < 4; ++g) {
                uint2 o;
                o.x = pack2(acc[4 * g], acc[4 * g + 1]);
                o.y = pack2(acc[4 * g + 2], acc[4 * g + 3]);
                *(uint2*)(stg + r * 144 + 16 * g + 8 * h) = o;
            }
        }
        if (c2 < 5) {
            const int c = 2 * c2 + 2;
#pragma unroll
            for (int ks = 0; ks < 8; ++ks) {
                int off = W2_BASE + (16 * ks + 4 * h + q) * W2_STRIDE + (c >> 2) * 256 + (((c & 3) * 64) ^ colb);
                fa[ks] = tr_frag(lds, off, off + 8 * W2_STRIDE);
            }
        }
        {
            f32x16 acc;
#pragma unroll
            for (int i = 0; i < 16; ++i) acc[i] = 0.f;
#pragma unroll
            for (int ks = 0; ks < 8; ++ks) acc = __builtin_amdgcn_mfma_f32_32x32x16_bf16(fb[ks], hf[ks], acc, 0, 0, 0);
#pragma unroll
            for (int g = 0; g < 4; ++g) {
                uint2 o;
                o.x = pack2(acc[4 * g], acc[4 * g + 1]);
                o.y = pack2(acc[4 * g + 2], acc[4 * g + 3]);
                *(uint2*)(stg + r * 144 + 64 + 16 * g + 8 * h) = o;
            }
        }
        __builtin_amdgcn_wave_barrier();
        u32x4 v[4];
#pragma unroll
        for (int i = 0; i < 4; ++i) v[i] = *(const u32x4*)(stg + (rrow + 8 * i) * 144 + rch * 16);
#pragma unroll
        for (int i = 0; i < 4; ++i) {
            int nd = tile * 32 + rrow + 8 * i;
            __builtin_amdgcn_raw_buffer_store_b128(v[i], rphi, (unsigned)nd * 768u + c2 * 128 + rch * 16, 0, 16);
        }
        __builtin_amdgcn_wave_barrier();
    }
}

struct RowData { unsigned ps, pvv, pvr, vx, vy, vz; };

typedef __attribute__((address_space(1))) char gchar;
typedef __attribute__((address_space(1))) unsigned gu32;
DI gchar* uptr(const void* p) {
    const unsigned long long v = (unsigned long long)p;
    const unsigned lo = __builtin_amdgcn_readfirstlane((unsigned)v);
    const unsigned hi = __builtin_amdgcn_readfirstlane((unsigned)(v >> 32));
    return (gchar*)(((unsigned long long)hi << 32) | lo);
}

DI void load_rows(const u16* __restrict__ sv, const u16* __restrict__ phi, int s, int lane, RowData& r) {
    const unsigned lo4 = (unsigned)lane * 4u;
    const gchar* p = uptr(phi + (size_t)s * 384);
    const gchar* v = uptr(sv + (size_t)s * 512);
    r.ps = *(const gu32*)(p + lo4);
    r.pvv = *(const gu32*)(p + lo4 + 256);
    r.pvr = *(const gu32*)(p + lo4 + 512);
    r.vx = *(const gu32*)(v + lo4 + 256);
    r.vy = *(const gu32*)(v + lo4 + 512);
    r.vz = *(const gu32*)(v + lo4 + 768);
}

typedef __attribute__((ext_vector_type(4))) float f32x4;
typedef __attribute__((ext_vector_type(2))) unsigned u32x2;

DI void accum_edge(const RowData& r, const float (&wf)[6], float hx, float hy, float hz, float (&acc)[8]) {
    acc[0] += lo16(r.ps) * wf[0];
    acc[1] += hi16(r.ps) * wf[1];
    float mvv0 = lo16(r.pvv) * wf[2], mvv1 = hi16(r.pvv) * wf[3];
    float mvr0 = lo16(r.pvr) * wf[4], mvr1 = hi16(r.pvr) * wf[5];
    acc[2] += mvv0 * lo16(r.vx) + mvr0 * hx;
    acc[3] += mvv1 * hi16(r.vx) + mvr1 * hx;
    acc[4] += mvv0 * lo16(r.vy) + mvr0 * hy;
    acc[5] += mvv1 * hi16(r.vy) + mvr1 * hy;
    acc[6] += mvv0 * lo16(r.vz) + mvr0 * hz;
    acc[7] += mvv1 * hi16(r.vz) + mvr1 * hz;
}

DI void wf_mfma(const u32x4& a0, const u32x4& a1, const u32x2* __restrict__ wl, int lane, unsigned (&P)[3][4]) {
    const u32x2 aq[4] = {{a0[0], a0[1]}, {a0[2], a0[3]}, {a1[0], a1[1]}, {a1[2], a1[3]}};
#pragma unroll
    for (int seg = 0; seg < 3; ++seg) {
        f32x4 d0 = {0.f, 0.f, 0.f, 0.f}, d1 = {0.f, 0.f, 0.f, 0.f};
#pragma unroll
        for (int q = 0; q < 4; ++q) {
            d0 = __builtin_amdgcn_mfma_f32_4x4x4bf16_1k(__builtin_bit_cast(s16x4, aq[q]),
                                                       __builtin_bit_cast(s16x4, wl[((2 * seg) * 4 + q) * 64 + lane]),
                                                       d0, 0, 0, 0);
            d1 = __builtin_amdgcn_mfma_f32_4x4x4bf16_1k(
                __builtin_bit_cast(s16x4, aq[q]), __builtin_bit_cast(s16x4, wl[((2 * seg + 1) * 4 + q) * 64 + lane]),
                d1, 0, 0, 0);
        }
#pragma unroll
        for (int u = 0; u < 4; ++u) P[seg][u] = pack2(d0[u], d1[u]);
        __builtin_amdgcn_sched_barrier(0);
    }
}

struct Chunk {
    u32x4 a0, a1, a2;
    RowData rd[4];
};

DI void chunk_load(const u16* __restrict__ sv, const u16* __restrict__ phi, const u32x4* __restrict__ ent_node,
                   int lane, int c, int base, int rank, int sly, Chunk& ck) {
    int idx[4], sr[4];
#pragma unroll
    for (int u = 0; u < 4; ++u) {
        const int rr = base + u < c ? base + u : base;
        idx[u] = __builtin_ctzll(__ballot(lane < c && rank == rr) | (1ull << 63));
        sr[u] = __builtin_amdgcn_readlane(sly, idx[u]);
    }
    const int u4 = lane & 3;
    int myidx = idx[0];
    myidx = u4 == 1 ? idx[1] : myidx;
    myidx = u4 == 2 ? idx[2] : myidx;
    myidx = u4 == 3 ? idx[3] : myidx;
    const u32x4* ep = ent_node + myidx * 4;
    ck.a0 = ep[0]; ck.a1 = ep[1]; ck.a2 = ep[2];
#pragma unroll
    for (int u = 0; u < 4; ++u) load_rows(sv, phi, sr[u], lane, ck.rd[u]);
}

DI void chunk_compute(const Chunk& ck, const u32x2* __restrict__ wl, int lane, int c, int base, float (&acc)[8]) {
    unsigned P[3][4];
    int lofs = lane;
    asm volatile("" : "+v"(lofs));
    wf_mfma(ck.a0, ck.a1, wl, lofs, P);
#pragma unroll
    for (int u = 0; u < 4; ++u) {
        if (u == 0 || base + u < c) {
            const float fc = __uint_as_float(__builtin_amdgcn_readlane(ck.a2[0], u));
            const float hx = __uint_as_float(__builtin_amdgcn_readlane(ck.a2[1], u));
            const float hy = __uint_as_float(__builtin_amdgcn_readlane(ck.a2[2], u));
            const float hz = __uint_as_float(__builtin_amdgcn_readlane(ck.a2[3], u));
            float wf[6];
#pragma unroll
            for (int sg = 0; sg < 3; ++sg) {
                wf[2 * sg] = lo16(P[sg][u]) * fc;
                wf[2 * sg + 1] = hi16(P[sg][u]) * fc;
            }
            accum_edge(ck.rd[u], wf, hx, hy, hz, acc);
        }
    }
}

#define GOCC 5
__global__ __launch_bounds__(256, GOCC) void k_gather(const u16* __restrict__ sv, const u16* __restrict__ phi,
                                                const int* __restrict__ cnt, const int2* __restrict__ slots,
                                                const u32x4* __restrict__ entries, const u32x4* __restrict__ wr,
                                                const int4* __restrict__ ovf_meta,
                                                const u32x4* __restrict__ ovf_entries,
                                                u16* __restrict__ out, int N, int cap) {
    __shared__ __attribute__((aligned(16))) u32x4 wlds[768];
    const int lane = threadIdx.x & 63;
    const int wave = __builtin_amdgcn_readfirstlane(blockIdx.x * 4 + (threadIdx.x >> 6));
    const int nw = gridDim.x * 4;
#pragma unroll
    for (int i = 0; i < 3; ++i) wlds[i * 256 + threadIdx.x] = wr[i * 256 + threadIdx.x];
    const u32x2* wl = (const u32x2*)wlds;

    int node = wave;
    const int n0 = node < N ? node : N - 1;
    int c_next = cnt[n0];
    int2 sl_next = slots[(size_t)n0 * cap + (lane & 15)];
    __syncthreads();
    bool any_big = false;
    for (; node < N; node += nw) {
        const int c = __builtin_amdgcn_readfirstlane(c_next);
        const int2 sl = sl_next;
        const int nn = node + nw < N ? node + nw : N - 1;
        const gu32* own = (const gu32*)(uptr(sv + (size_t)node * 512) + (unsigned)lane * 4u);
        gu32* op = (gu32*)(uptr(out + (size_t)node * 512) + (unsigned)lane * 4u);
        if (c == 0 || c > 16) {
            if (c == 0) {
                const unsigned o0 = own[0], o1 = own[64], o2 = own[128], o3 = own[192];
                c_next = cnt[nn];
                sl_next = slots[(size_t)nn * cap + (lane & 15)];
                op[0] = o0; op[64] = o1; op[128] = o2; op[192] = o3;
            } else {
                any_big = true;
                c_next = cnt[nn];
                sl_next = slots[(size_t)nn * cap + (lane & 15)];
            }
            continue;
        }
        float acc[8];
#pragma unroll
        for (int i = 0; i < 8; ++i) acc[i] = 0.f;
        int rank = 0;
        for (int j = 0; j < c; ++j) {
            int ej = __builtin_amdgcn_readlane(sl.x, j);
            rank += (ej < sl.x) ? 1 : 0;
        }
        const u32x4* ent_node = entries + (size_t)node * cap * 4;
        unsigned o0, o1, o2, o3;
        {
            Chunk ck;
            chunk_load(sv, phi, ent_node, lane, c, 0, rank, sl.y, ck);
            o0 = own[0]; o1 = own[64]; o2 = own[128]; o3 = own[192];
            c_next = cnt[nn];
            sl_next = slots[(size_t)nn * cap + (lane & 15)];
            chunk_compute(ck, wl, lane, c, 0, acc);
        }
        for (int base = 4; base < c; base += 4) {
            Chunk ck;
            chunk_load(sv, phi, ent_node, lane, c, base, rank, sl.y, ck);
            chunk_compute(ck, wl, lane, c, base, acc);
        }
        op[0] = pack2(lo16(o0) + acc[0], hi16(o0) + acc[1]);
        op[64] = pack2(lo16(o1) + acc[2], hi16(o1) + acc[3]);
        op[128] = pack2(lo16(o2) + acc[4], hi16(o2) + acc[5]);
        op[192] = pack2(lo16(o3) + acc[6], hi16(o3) + acc[7]);
    }
    if (!any_big) return;

    for (node = wave; node < N; node += nw) {
        const int c = __builtin_amdgcn_readfirstlane(cnt[node]);
        if (c <= 16) continue;
        int2 sl = make_int2(0, 0);
        if (lane < cap) sl = slots[(size_t)node * cap + lane];
        const unsigned* own = (const unsigned*)(sv + (size_t)node * 512) + lane;
        unsigned o0 = own[0], o1 = own[64], o2 = own[128], o3 = own[192];
        float acc[8];
#pragma unroll
        for (int i = 0; i < 8; ++i) acc[i] = 0.f;
        const int L = (c > cap) ? cnt[N] : 0;
        const int nb = c < cap ? c : cap;
        int last = -1;
        for (int it = 0; it < c; ++it) {
            int best = 0x7fffffff, loc = 0, bs = 0;
            if (lane < nb && sl.x > last) { best = sl.x; loc = lane; bs = sl.y; }
            for (int i = lane; i < L; i += 64) {
                int4 o = ovf_meta[i];
                if (o.w == node && o.x > last && o.x < best) { best = o.x; loc = -1 - i; bs = o.y; }
            }
            int mn = best;
#pragma unroll
            for (int sft = 32; sft >= 1; sft >>= 1) {
                int other = __shfl_xor(mn, sft, 64);
                mn = other < mn ? other : mn;
            }
            unsigned long long m = __ballot(best == mn);
            int owner = __builtin_ctzll(m);
            int l2 = __builtin_amdgcn_readlane(loc, owner);
            int s = __builtin_amdgcn_readlane(bs, owner);
            const u32x4* ep = (l2 >= 0) ? entries + ((size_t)node * cap + l2) * 4
                                        : ovf_entries + (size_t)(-1 - l2) * 4;
            RowData rd;
            load_rows(sv, phi, s, lane, rd);
            u32x4 a0 = ep[0], a1 = ep[1], a2 = ep[2];
            unsigned P[3][4];
            int lofs = lane;
            asm volatile("" : "+v"(lofs));
            wf_mfma(a0, a1, wl, lofs, P);
            float wf[6];
#pragma unroll
            for (int sg = 0; sg < 3; ++sg) {
                wf[2 * sg] = lo16(P[sg][0]) * __uint_as_float(a2[0]);
                wf[2 * sg + 1] = hi16(P[sg][0]) * __uint_as_float(a2[0]);
            }
            accum_edge(rd, wf, __uint_as_float(a2[1]), __uint_as_float(a2[2]), __uint_as_float(a2[3]), acc);
            last = mn;
        }
        unsigned* op = (unsigned*)(out + (size_t)node * 512) + lane;
        op[0] = pack2(lo16(o0) + acc[0], hi16(o0) + acc[1]);
        op[64] = pack2(lo16(o1) + acc[2], hi16(o1) + acc[3]);
        op[128] = pack2(lo16(o2) + acc[4], hi16(o2) + acc[5]);
        op[192] = pack2(lo16(o3) + acc[6], hi16(o3) + acc[7]);
    }
}

static inline size_t align_up(size_t x, size_t a) { return (x + a - 1) / a * a; }

#define GBLOCKS 1280

extern "C" void kernel_launch(void* const* d_in, const int* in_sizes, int n_in, void* d_out, int out_size,
                              void* d_ws, size_t ws_size, hipStream_t stream) {
    const u16* sv = (const u16*)d_in[0];
    const u16* pos = (const u16*)d_in[1];
    const int* src = (const int*)d_in[2];
    const int* dst = (const int*)d_in[3];
    const u16* freqs = (const u16*)d_in[4];
    const u16* w1 = (const u16*)d_in[5];
    const u16* w2 = (const u16*)d_in[6];
    const u16* wrbf = (const u16*)d_in[7];
    u16* out = (u16*)d_out;
    const int N = in_sizes[1] / 3;
    const int E = in_sizes[2];

#define CAPV 32
    int cap = CAPV;
    size_t off_cnt, off_wr, off_phi, off_slots, off_ent, off_om, off_oe, total;
    for (;;) {
        size_t o = 0;
        off_cnt = o; o = align_up(o + (size_t)(N + 1) * 4, 256);
        off_wr = o; o = align_up(o + 48 * 64 * 4, 256);
        off_phi = o; o = align_up(o + (size_t)N * 384 * 2, 256);
        off_slots = o; o = align_up(o + (size_t)N * cap * 8, 256);
        off_ent = o; o = align_up(o + (size_t)N * cap * 64, 256);
        off_om = o; o = align_up(o + (size_t)E * 16, 256);
        off_oe = o; o = align_up(o + (size_t)E * 64, 256);
        total = o;
        if (total <= ws_size || cap <= 16) break;
        cap >>= 1;
    }
    char* ws = (char*)d_ws;
    int* cnt = (int*)(ws + off_cnt);
    unsigned* wr = (unsigned*)(ws + off_wr);
    u16* phi = (u16*)(ws + off_phi);
    int2* slots = (int2*)(ws + off_slots);
    u32x4* entries = (u32x4*)(ws + off_ent);
    int4* ovf_meta = (int4*)(ws + off_om);
    u32x4* ovf_entries = (u32x4*)(ws + off_oe);

    const int tiles = (N + 31) / 32;
    const int front_blocks = (tiles + 3) / 4;
    (void)hipMemsetAsync(cnt, 0, align_up((size_t)(N + 1) * 4, 16), stream);
    const int fgrid = front_blocks < 256 ? 256 : front_blocks;
    k_front<<<fgrid, 512 + 64 * PFW, 0, stream>>>(sv, w1, w2, wrbf, phi, wr, pos, src, dst, freqs, cnt, slots, entries, ovf_meta,
                                       ovf_entries, N, E, cap, front_blocks);
    int gblocks = (N + 3) / 4;
    if (gblocks > GBLOCKS) gblocks = GBLOCKS;
    k_gather<<<gblocks, 256, 0, stream>>>(sv, phi, cnt, slots, entries, (const u32x4*)wr, ovf_meta, ovf_entries, out,
                                          N, cap);
}
```

```cpp
#include <hip/hip_runtime.h>
#include <hip/hip_bf16.h>
#include <stdint.h>

typedef __attribute__((ext_vector_type(8))) short bf16x8;
typedef __attribute__((ext_vector_type(16))) float f32x16;
typedef __attribute__((ext_vector_type(4))) unsigned u32x4;
typedef __attribute__((ext_vector_type(2))) __bf16 bf16x2_t;
typedef __attribute__((ext_vector_type(2))) float f32x2_t;
typedef unsigned short u16;

#define DI __device__ __forceinline__

DI float bf2f(u16 x) { return __uint_as_float(((unsigned)x) << 16); }
DI unsigned pack2(float a, float b) {
    f32x2_t v = {a, b};
    bf16x2_t r = __builtin_convertvector(v, bf16x2_t);
    return __builtin_bit_cast(unsigned, r);
}
DI float rb(float a) {
    return __uint_as_float(pack2(a, 0.f) << 16);
}
DI float lo16(unsigned u) { return __uint_as_float(u << 16); }
DI float hi16(unsigned u) { return __uint_as_float(u & 0xffff0000u); }

struct EdgeGeom {
    unsigned rbf[8];
    float fcut, rx, ry, rz;
};

struct __attribute__((aligned(4))) Pos8 { unsigned a, b; };
DI void load_pos(const u16* __restrict__ pos, int i, int last8, float& x, float& y, float& z) {
    const int byte = 6 * i;
    const int w = byte & ~3;
    if (w <= last8) {
        const Pos8 v = *(const Pos8*)((const char*)pos + w);
        const unsigned long long q = (((unsigned long long)v.b << 32) | v.a) >> ((byte - w) * 8);
        x = __uint_as_float((unsigned)q << 16);
        y = __uint_as_float((unsigned)q & 0xffff0000u);
        z = __uint_as_float((unsigned)(q >> 32) << 16);
    } else {
        x = bf2f(pos[3 * i]); y = bf2f(pos[3 * i + 1]); z = bf2f(pos[3 * i + 2]);
    }
}
DI bool edge_dist(float sx, float sy, float sz, float tx, float ty, float tz, float& rx, float& ry, float& rz,
                  float& d) {
    rx = rb(sx - tx); ry = rb(sy - ty); rz = rb(sz - tz);
    float q = rb(rb(rx * rx) + rb(ry * ry) + rb(rz * rz));
    d = rb(sqrtf(rb(q + 1e-08f)));
    return d < 5.0f;
}
template <bool SLOW>
DI void edge_rest(const unsigned (&fq)[8], float rx, float ry, float rz, float d, EdgeGeom& g) {
    const float inv = 1.0f / d;
    g.rx = rb(rx * inv); g.ry = rb(ry * inv); g.rz = rb(rz * inv);
    const float ca = rb(rb(3.141592653589793f * d) / 5.0f);
    float c = rb(__builtin_amdgcn_cosf(ca * 0.15915494309189535f));
    g.fcut = rb(0.5f * rb(c + 1.0f));
#pragma unroll
    for (int kp = 0; kp < 8; ++kp) {
        float x0 = rb(d * lo16(fq[kp])), x1 = rb(d * hi16(fq[kp]));
        float s0, s1;
        if (SLOW) {
            s0 = sinf(x0); s1 = sinf(x1);
        } else {
            s0 = __builtin_amdgcn_sinf(x0 * 0.15915494309189535f);
            s1 = __builtin_amdgcn_sinf(x1 * 0.15915494309189535f);
        }
        g.rbf[kp] = pack2(rb(rb(s0) * inv), rb(rb(s1) * inv));
    }
}

template <int EU, bool SLOW>
DI void edge_loop(const u16* __restrict__ pos, const int* __restrict__ src, const int* __restrict__ dst,
                  const unsigned (&fq)[8], int* __restrict__ cnt, int2* __restrict__ slots,
                  u32x4* __restrict__ entries, int4* __restrict__ ovf_meta, u32x4* __restrict__ ovf_entries, int N,
                  int E, int cap, int ew, int nw, int lane) {
    const int nchunks = (E + 63) >> 6;
    const int last8 = 6 * N - 8;
    for (int c0 = ew; c0 < nchunks; c0 += nw * EU) {
        int es[EU], ss[EU], ts[EU];
#pragma unroll
        for (int u = 0; u < EU; ++u) {
            int e = (c0 + u * nw) * 64 + lane;
            es[u] = e;
            int ec = e < E ? e : E - 1;
            ss[u] = src[ec]; ts[u] = dst[ec];
        }
        float rx[EU], ry[EU], rz[EU], d[EU];
        float px[EU], py[EU], pz[EU];
        bool act[EU];
#pragma unroll
        for (int u = 0; u < EU; ++u) {
            load_pos(pos, ss[u], last8, px[u], py[u], pz[u]);
            load_pos(pos, ts[u], last8, rx[u], ry[u], rz[u]);
        }
#pragma unroll
        for (int u = 0; u < EU; ++u)
            act[u] = edge_dist(px[u], py[u], pz[u], rx[u], ry[u], rz[u], rx[u], ry[u], rz[u], d[u]) && es[u] < E;
        int slot[EU];
#pragma unroll
        for (int u = 0; u < EU; ++u) slot[u] = act[u] ? atomicAdd(&cnt[ts[u]], 1) : 0;
#pragma unroll
        for (int u = 0; u < EU; ++u) {
            if (act[u]) {
                EdgeGeom g;
                edge_rest<SLOW>(fq, rx[u], ry[u], rz[u], d[u], g);
                u32x4* ep;
                if (slot[u] < cap) {
                    const size_t idx = (size_t)ts[u] * cap + slot[u];
                    slots[idx] = make_int2(es[u], ss[u]);
                    ep = entries + idx * 4;
                } else {
                    const int o = atomicAdd(&cnt[N], 1);
                    ovf_meta[o] = make_int4(es[u], ss[u], o, ts[u]);
                    ep = ovf_entries + (size_t)o * 4;
                }
                u32x4 a = {g.rbf[0], g.rbf[1], g.rbf[2], g.rbf[3]};
                u32x4 b = {g.rbf[4], g.rbf[5], g.rbf[6], g.rbf[7]};
                u32x4 c = {__float_as_uint(g.fcut), __float_as_uint(g.rx), __float_as_uint(g.ry),
                           __float_as_uint(g.rz)};
                ep[0] = a;
                ep[1] = b;
                ep[2] = c;
            }
        }
    }
}

typedef __attribute__((ext_vector_type(4))) short s16x4;
typedef __attribute__((address_space(3))) s16x4 lds_s16x4;

#define EUV 8
constexpr int W1_STRIDE = 256;
constexpr int W2_STRIDE = 768;
constexpr int W2_BASE = 128 * W1_STRIDE;
constexpr int STG_BASE = W2_BASE + 128 * W2_STRIDE;
constexpr int FRONT_LDS = STG_BASE + 4 * 32 * 144 + 16;

DI bf16x8 tr_frag(const char* lds, int off0, int off1) {
    s16x4 a = __builtin_amdgcn_ds_read_tr16_b64_v4i16((lds_s16x4*)(lds + off0));
    s16x4 b = __builtin_amdgcn_ds_read_tr16_b64_v4i16((lds_s16x4*)(lds + off1));
    return __builtin_shufflevector(a, b, 0, 1, 2, 3, 4, 5, 6, 7);
}

__global__ __launch_bounds__(512) void k_front(const u16* __restrict__ sv, const u16* __restrict__ w1,
                                               const u16* __restrict__ w2, const u16* __restrict__ wrbf,
                                               u16* __restrict__ phi, unsigned* __restrict__ wr,
                                               const u16* __restrict__ pos, const int* __restrict__ src,
                                               const int* __restrict__ dst, const u16* __restrict__ freqs,
                                               int* __restrict__ cnt, int2* __restrict__ slots,
                                               u32x4* __restrict__ entries, int4* __restrict__ ovf_meta,
                                               u32x4* __restrict__ ovf_entries, int N, int E, int cap,
                                               int gemm_blocks) {
    __shared__ __attribute__((aligned(16))) char lds[FRONT_LDS];
    const int tid = threadIdx.x;
    const int lane = tid & 63;
    const int wv = __builtin_amdgcn_readfirstlane(tid >> 6);
    const bool gemm_block = (int)blockIdx.x < gemm_blocks;
    int* bar = (int*)(lds + FRONT_LDS - 16);
    if (tid == 0) *bar = 0;
    __syncthreads();

    if (wv >= 4 || !gemm_block) {
        const int ew = gemm_block ? (int)blockIdx.x * 4 + (wv - 4)
                                  : gemm_blocks * 4 + ((int)blockIdx.x - gemm_blocks) * 8 + wv;
        const int nw = gemm_blocks * 4 + ((int)gridDim.x - gemm_blocks) * 8;
        unsigned fq[8];
#pragma unroll
        for (int kp = 0; kp < 8; ++kp) fq[kp] = (unsigned)freqs[2 * kp] | ((unsigned)freqs[2 * kp + 1] << 16);
        for (int i = ew * 64 + lane; i < 48 * 64; i += nw * 64) {
            int half = i & 1, ln = (i >> 1) & 63, gq = i >> 7;
            int g = gq >> 2, q = gq & 3;
            int ch = (g >> 1) * 128 + 2 * ln + (g & 1);
            int k = 4 * q + 2 * half;
            unsigned a = wrbf[k * 384 + ch], b = wrbf[(k + 1) * 384 + ch];
            wr[i] = a | (b << 16);
        }
        float fmax = 0.f;
#pragma unroll
        for (int kp = 0; kp < 8; ++kp) fmax = fmaxf(fmax, fmaxf(fabsf(lo16(fq[kp])), fabsf(hi16(fq[kp]))));
        if (fmax * 5.0f <= 60.0f)
            edge_loop<EUV, false>(pos, src, dst, fq, cnt, slots, entries, ovf_meta, ovf_entries, N, E, cap, ew, nw, lane);
        else
            edge_loop<1, true>(pos, src, dst, fq, cnt, slots, entries, ovf_meta, ovf_entries, N, E, cap, ew, nw, lane);
        return;
    }

    const int r = lane & 31, h = lane >> 5;
    const int tile = blockIdx.x * 4 + wv;
    const int node = tile * 32 + r;
    const bool valid = node < N;
    bf16x8 sf[8];
    {
        const bf16x8* srow = (const bf16x8*)(sv + (size_t)(valid ? node : 0) * 512);
#pragma unroll
        for (int ks = 0; ks < 8; ++ks) sf[ks] = srow[2 * ks + h];
    }
    {
        const u32x4* g1 = (const u32x4*)w1;
#pragma unroll
        for (int i = 0; i < 8; ++i) {
            int c = i * 256 + tid;
            int row = c >> 4, ch = c & 15;
            *(u32x4*)(lds + row * W1_STRIDE + ((ch * 16) ^ ((row & 3) << 6))) = g1[c];
        }
        const u32x4* g2 = (const u32x4*)w2;
#pragma unroll
        for (int i = 0; i < 24; ++i) {
            int c = i * 256 + tid;
            int row = c / 48, ch = c % 48;
            *(u32x4*)(lds + W2_BASE + row * W2_STRIDE + ((ch * 16) ^ ((row & 3) << 6))) = g2[c];
        }
    }
    __builtin_amdgcn_fence(__ATOMIC_RELEASE, "workgroup");
    if (lane == 0) __hip_atomic_fetch_add(bar, 1, __ATOMIC_RELAXED, __HIP_MEMORY_SCOPE_WORKGROUP);
    while (__hip_atomic_load(bar, __ATOMIC_RELAXED, __HIP_MEMORY_SCOPE_WORKGROUP) < 4) __builtin_amdgcn_s_sleep(1);
    __builtin_amdgcn_fence(__ATOMIC_ACQUIRE, "workgroup");

    const int i16 = lane & 15, q = i16 >> 2, p4 = i16 & 3, g1b = (lane >> 4) & 1;
    const int colb = ((16 * g1b + 4 * p4) * 2) ^ (q << 6);
    bf16x8 hf[8];
#pragma unroll
    for (int t = 0; t < 4; ++t) {
        bf16x8 fr[8];
#pragma unroll
        for (int ks = 0; ks < 8; ++ks) {
            int off = (16 * ks + 8 * h + q) * W1_STRIDE + ((64 * t) ^ colb);
            fr[ks] = tr_frag(lds, off, off + 4 * W1_STRIDE);
        }
        f32x16 acc;
#pragma unroll
        for (int i = 0; i < 16; ++i) acc[i] = 0.f;
#pragma unroll
        for (int ks = 0; ks < 8; ++ks) acc = __builtin_amdgcn_mfma_f32_32x32x16_bf16(fr[ks], sf[ks], acc, 0, 0, 0);
        unsigned p[8];
#pragma unroll
        for (int i = 0; i < 8; ++i) {
            float x0 = rb(acc[2 * i]), x1 = rb(acc[2 * i + 1]);
            float y0 = x0 * __builtin_amdgcn_rcpf(1.0f + __expf(-x0)), y1 = x1 * __builtin_amdgcn_rcpf(1.0f + __expf(-x1));
            p[i] = pack2(y0, y1);
        }
        u32x4 a = {p[0], p[1], p[2], p[3]}, b = {p[4], p[5], p[6], p[7]};
        hf[2 * t] = __builtin_bit_cast(bf16x8, a);
        hf[2 * t + 1] = __builtin_bit_cast(bf16x8, b);
    }
    char* stg = lds + STG_BASE + wv * (32 * 144);
    const int rrow = lane >> 3, rch = lane & 7;
    const __amdgpu_buffer_rsrc_t rphi = __builtin_amdgcn_make_buffer_rsrc((void*)phi, 0, N * 768, 0x00020000);
    bf16x8 fa[8], fb[8];
#pragma unroll
    for (int ks = 0; ks < 8; ++ks) {
        int off = W2_BASE + (16 * ks + 4 * h + q) * W2_STRIDE + colb;
        fa[ks] = tr_frag(lds, off, off + 8 * W2_STRIDE);
    }
#pragma unroll
    for (int c2 = 0; c2 < 6; ++c2) {
        {
            const int c = 2 * c2 + 1;
#pragma unroll
            for (int ks = 0; ks < 8; ++ks) {
                int off = W2_BASE + (16 * ks + 4 * h + q) * W2_STRIDE + (c >> 2) * 256 + (((c & 3) * 64) ^ colb);
                fb[ks] = tr_frag(lds, off, off + 8 * W2_STRIDE);
            }
        }
        {
            f32x16 acc;
#pragma unroll
            for (int i = 0; i < 16; ++i) acc[i] = 0.f;
#pragma unroll
            for (int ks = 0; ks < 8; ++ks) acc = __builtin_amdgcn_mfma_f32_32x32x16_bf16(fa[ks], hf[ks], acc, 0, 0, 0);
#pragma unroll
            for (int g = 0; g < 4; ++g) {
                uint2 o;
                o.x = pack2(acc[4 * g], acc[4 * g + 1]);
                o.y = pack2(acc[4 * g + 2], acc[4 * g + 3]);
                *(uint2*)(stg + r * 144 + 16 * g + 8 * h) = o;
            }
        }
        if (c2 < 5) {
            const int c = 2 * c2 + 2;
#pragma unroll
            for (int ks = 0; ks < 8; ++ks) {
                int off = W2_BASE + (16 * ks + 4 * h + q) * W2_STRIDE + (c >> 2) * 256 + (((c & 3) * 64) ^ colb);
                fa[ks] = tr_frag(lds, off, off + 8 * W2_STRIDE);
            }
        }
        {
            f32x16 acc;
#pragma unroll
            for (int i = 0; i < 16; ++i) acc[i] = 0.f;
#pragma unroll
            for (int ks = 0; ks < 8; ++ks) acc = __builtin_amdgcn_mfma_f32_32x32x16_bf16(fb[ks], hf[ks], acc, 0, 0, 0);
#pragma unroll
            for (int g = 0; g < 4; ++g) {
                uint2 o;
                o.x = pack2(acc[4 * g], acc[4 * g + 1]);
                o.y = pack2(acc[4 * g + 2], acc[4 * g + 3]);
                *(uint2*)(stg + r * 144 + 64 + 16 * g + 8 * h) = o;
            }
        }
        __builtin_amdgcn_wave_barrier();
        u32x4 v[4];
#pragma unroll
        for (int i = 0; i < 4; ++i) v[i] = *(const u32x4*)(stg + (rrow + 8 * i) * 144 + rch * 16);
#pragma unroll
        for (int i = 0; i < 4; ++i) {
            int nd = tile * 32 + rrow + 8 * i;
            __builtin_amdgcn_raw_buffer_store_b128(v[i], rphi, (unsigned)nd * 768u + c2 * 128 + rch * 16, 0, 16);
        }
        __builtin_amdgcn_wave_barrier();
    }
}

struct RowData { unsigned ps, pvv, pvr, vx, vy, vz; };

typedef __attribute__((address_space(1))) char gchar;
typedef __attribute__((address_space(1))) unsigned gu32;
DI gchar* uptr(const void* p) {
    const unsigned long long v = (unsigned long long)p;
    const unsigned lo = __builtin_amdgcn_readfirstlane((unsigned)v);
    const unsigned hi = __builtin_amdgcn_readfirstlane((unsigned)(v >> 32));
    return (gchar*)(((unsigned long long)hi << 32) | lo);
}

DI void load_rows(const u16* __restrict__ sv, const u16* __restrict__ phi, int s, int lane, RowData& r) {
    const unsigned lo4 = (unsigned)lane * 4u;
    const gchar* p = uptr(phi + (size_t)s * 384);
    const gchar* v = uptr(sv + (size_t)s * 512);
    r.ps = *(const gu32*)(p + lo4);
    r.pvv = *(const gu32*)(p + lo4 + 256);
    r.pvr = *(const gu32*)(p + lo4 + 512);
    r.vx = *(const gu32*)(v + lo4 + 256);
    r.vy = *(const gu32*)(v + lo4 + 512);
    r.vz = *(const gu32*)(v + lo4 + 768);
}

typedef __attribute__((ext_vector_type(4))) float f32x4;
typedef __attribute__((ext_vector_type(2))) unsigned u32x2;

DI void accum_edge(const RowData& r, const float (&wf)[6], float hx, float hy, float hz, float (&acc)[8]) {
    acc[0] += lo16(r.ps) * wf[0];
    acc[1] += hi16(r.ps) * wf[1];
    float mvv0 = lo16(r.pvv) * wf[2], mvv1 = hi16(r.pvv) * wf[3];
    float mvr0 = lo16(r.pvr) * wf[4], mvr1 = hi16(r.pvr) * wf[5];
    acc[2] += mvv0 * lo16(r.vx) + mvr0 * hx;
    acc[3] += mvv1 * hi16(r.vx) + mvr1 * hx;
    acc[4] += mvv0 * lo16(r.vy) + mvr0 * hy;
    acc[5] += mvv1 * hi16(r.vy) + mvr1 * hy;
    acc[6] += mvv0 * lo16(r.vz) + mvr0 * hz;
    acc[7] += mvv1 * hi16(r.vz) + mvr1 * hz;
}

DI void wf_mfma(const u32x4& a0, const u32x4& a1, const u32x2* __restrict__ wl, int lane, unsigned (&P)[3][4]) {
    const u32x2 aq[4] = {{a0[0], a0[1]}, {a0[2], a0[3]}, {a1[0], a1[1]}, {a1[2], a1[3]}};
#pragma unroll
    for (int seg = 0; seg < 3; ++seg) {
        f32x4 d0 = {0.f, 0.f, 0.f, 0.f}, d1 = {0.f, 0.f, 0.f, 0.f};
#pragma unroll
        for (int q = 0; q < 4; ++q) {
            d0 = __builtin_amdgcn_mfma_f32_4x4x4bf16_1k(__builtin_bit_cast(s16x4, aq[q]),
                                                       __builtin_bit_cast(s16x4, wl[((2 * seg) * 4 + q) * 64 + lane]),
                                                       d0, 0, 0, 0);
            d1 = __builtin_amdgcn_mfma_f32_4x4x4bf16_1k(
                __builtin_bit_cast(s16x4, aq[q]), __builtin_bit_cast(s16x4, wl[((2 * seg + 1) * 4 + q) * 64 + lane]),
                d1, 0, 0, 0);
        }
#pragma unroll
        for (int u = 0; u < 4; ++u) P[seg][u] = pack2(d0[u], d1[u]);
        __builtin_amdgcn_sched_barrier(0);
    }
}

struct Chunk {
    u32x4 a0, a1, a2;
    RowData rd[4];
};

DI void chunk_load(const u16* __restrict__ sv, const u16* __restrict__ phi, const u32x4* __restrict__ ent_node,
                   int lane, int c, int base, int rank, int sly, Chunk& ck) {
    int idx[4], sr[4];
#pragma unroll
    for (int u = 0; u < 4; ++u) {
        const int rr = base + u < c ? base + u : base;
        idx[u] = __builtin_ctzll(__ballot(lane < c && rank == rr) | (1ull << 63));
        sr[u] = __builtin_amdgcn_readlane(sly, idx[u]);
    }
    const int u4 = lane & 3;
    int myidx = idx[0];
    myidx = u4 == 1 ? idx[1] : myidx;
    myidx = u4 == 2 ? idx[2] : myidx;
    myidx = u4 == 3 ? idx[3] : myidx;
    const u32x4* ep = ent_node + myidx * 4;
    ck.a0 = ep[0]; ck.a1 = ep[1]; ck.a2 = ep[2];
#pragma unroll
    for (int u = 0; u < 4; ++u) load_rows(sv, phi, sr[u], lane, ck.rd[u]);
}

DI void chunk_compute(const Chunk& ck, const u32x2* __restrict__ wl, int lane, int c, int base, float (&acc)[8]) {
    unsigned P[3][4];
    int lofs = lane;
    asm volatile("" : "+v"(lofs));
    wf_mfma(ck.a0, ck.a1, wl, lofs, P);
#pragma unroll
    for (int u = 0; u < 4; ++u) {
        if (u == 0 || base + u < c) {
            const float fc = __uint_as_float(__builtin_amdgcn_readlane(ck.a2[0], u));
            const float hx = __uint_as_float(__builtin_amdgcn_readlane(ck.a2[1], u));
            const float hy = __uint_as_float(__builtin_amdgcn_readlane(ck.a2[2], u));
            const float hz = __uint_as_float(__builtin_amdgcn_readlane(ck.a2[3], u));
            float wf[6];
#pragma unroll
            for (int sg = 0; sg < 3; ++sg) {
                wf[2 * sg] = lo16(P[sg][u]) * fc;
                wf[2 * sg + 1] = hi16(P[sg][u]) * fc;
            }
            accum_edge(ck.rd[u], wf, hx, hy, hz, acc);
        }
    }
}

#define GOCC 5
__global__ __launch_bounds__(256, GOCC) void k_gather(const u16* __restrict__ sv, const u16* __restrict__ phi,
                                                const int* __restrict__ cnt, const int2* __restrict__ slots,
                                                const u32x4* __restrict__ entries, const u32x4* __restrict__ wr,
                                                const int4* __restrict__ ovf_meta,
                                                const u32x4* __restrict__ ovf_entries,
                                                u16* __restrict__ out, int N, int cap) {
    __shared__ __attribute__((aligned(16))) u32x4 wlds[768];
    const int lane = threadIdx.x & 63;
    const int wave = __builtin_amdgcn_readfirstlane(blockIdx.x * 4 + (threadIdx.x >> 6));
    const int nw = gridDim.x * 4;
#pragma unroll
    for (int i = 0; i < 3; ++i) wlds[i * 256 + threadIdx.x] = wr[i * 256 + threadIdx.x];
    const u32x2* wl = (const u32x2*)wlds;

    int node = wave;
    const int n0 = node < N ? node : N - 1;
    int c_next = cnt[n0];
    int2 sl_next = slots[(size_t)n0 * cap + (lane & 15)];
    __syncthreads();
    bool any_big = false;
    for (; node < N; node += nw) {
        const int c = __builtin_amdgcn_readfirstlane(c_next);
        const int2 sl = sl_next;
        const int nn = node + nw < N ? node + nw : N - 1;
        const gu32* own = (const gu32*)(uptr(sv + (size_t)node * 512) + (unsigned)lane * 4u);
        gu32* op = (gu32*)(uptr(out + (size_t)node * 512) + (unsigned)lane * 4u);
        if (c == 0 || c > 16) {
            if (c == 0) {
                const unsigned o0 = own[0], o1 = own[64], o2 = own[128], o3 = own[192];
                c_next = cnt[nn];
                sl_next = slots[(size_t)nn * cap + (lane & 15)];
                op[0] = o0; op[64] = o1; op[128] = o2; op[192] = o3;
            } else {
                any_big = true;
                c_next = cnt[nn];
                sl_next = slots[(size_t)nn * cap + (lane & 15)];
            }
            continue;
        }
        float acc[8];
#pragma unroll
        for (int i = 0; i < 8; ++i) acc[i] = 0.f;
        int rank = 0;
        for (int j = 0; j < c; ++j) {
            int ej = __builtin_amdgcn_readlane(sl.x, j);
            rank += (ej < sl.x) ? 1 : 0;
        }
        const u32x4* ent_node = entries + (size_t)node * cap * 4;
        unsigned o0, o1, o2, o3;
        {
            Chunk ck;
            chunk_load(sv, phi, ent_node, lane, c, 0, rank, sl.y, ck);
            o0 = own[0]; o1 = own[64]; o2 = own[128]; o3 = own[192];
            c_next = cnt[nn];
            sl_next = slots[(size_t)nn * cap + (lane & 15)];
            chunk_compute(ck, wl, lane, c, 0, acc);
        }
        for (int base = 4; base < c; base += 4) {
            Chunk ck;
            chunk_load(sv, phi, ent_node, lane, c, base, rank, sl.y, ck);
            chunk_compute(ck, wl, lane, c, base, acc);
        }
        op[0] = pack2(lo16(o0) + acc[0], hi16(o0) + acc[1]);
        op[64] = pack2(lo16(o1) + acc[2], hi16(o1) + acc[3]);
        op[128] = pack2(lo16(o2) + acc[4], hi16(o2) + acc[5]);
        op[192] = pack2(lo16(o3) + acc[6], hi16(o3) + acc[7]);
    }
    if (!any_big) return;

    for (node = wave; node < N; node += nw) {
        const int c = __builtin_amdgcn_readfirstlane(cnt[node]);
        if (c <= 16) continue;
        int2 sl = make_int2(0, 0);
        if (lane < cap) sl = slots[(size_t)node * cap + lane];
        const unsigned* own = (const unsigned*)(sv + (size_t)node * 512) + lane;
        unsigned o0 = own[0], o1 = own[64], o2 = own[128], o3 = own[192];
        float acc[8];
#pragma unroll
        for (int i = 0; i < 8; ++i) acc[i] = 0.f;
        const int L = (c > cap) ? cnt[N] : 0;
        const int nb = c < cap ? c : cap;
        int last = -1;
        for (int it = 0; it < c; ++it) {
            int best = 0x7fffffff, loc = 0, bs = 0;
            if (lane < nb && sl.x > last) { best = sl.x; loc = lane; bs = sl.y; }
            for (int i = lane; i < L; i += 64) {
                int4 o = ovf_meta[i];
                if (o.w == node && o.x > last && o.x < best) { best = o.x; loc = -1 - i; bs = o.y; }
            }
            int mn = best;
#pragma unroll
            for (int sft = 32; sft >= 1; sft >>= 1) {
                int other = __shfl_xor(mn, sft, 64);
                mn = other < mn ? other : mn;
            }
            unsigned long long m = __ballot(best == mn);
            int owner = __builtin_ctzll(m);
            int l2 = __builtin_amdgcn_readlane(loc, owner);
            int s = __builtin_amdgcn_readlane(bs, owner);
            const u32x4* ep = (l2 >= 0) ? entries + ((size_t)node * cap + l2) * 4
                                        : ovf_entries + (size_t)(-1 - l2) * 4;
            RowData rd;
            load_rows(sv, phi, s, lane, rd);
            u32x4 a0 = ep[0], a1 = ep[1], a2 = ep[2];
            unsigned P[3][4];
            int lofs = lane;
            asm volatile("" : "+v"(lofs));
            wf_mfma(a0, a1, wl, lofs, P);
            float wf[6];
#pragma unroll
            for (int sg = 0; sg < 3; ++sg) {
                wf[2 * sg] = lo16(P[sg][0]) * __uint_as_float(a2[0]);
                wf[2 * sg + 1] = hi16(P[sg][0]) * __uint_as_float(a2[0]);
            }
            accum_edge(rd, wf, __uint_as_float(a2[1]), __uint_as_float(a2[2]), __uint_as_float(a2[3]), acc);
            last = mn;
        }
        unsigned* op = (unsigned*)(out + (size_t)node * 512) + lane;
        op[0] = pack2(lo16(o0) + acc[0], hi16(o0) + acc[1]);
        op[64] = pack2(lo16(o1) + acc[2], hi16(o1) + acc[3]);
        op[128] = pack2(lo16(o2) + acc[4], hi16(o2) + acc[5]);
        op[192] = pack2(lo16(o3) + acc[6], hi16(o3) + acc[7]);
    }
}

static inline size_t align_up(size_t x, size_t a) { return (x + a - 1) / a * a; }

#define GBLOCKS 1280

extern "C" void kernel_launch(void* const* d_in, const int* in_sizes, int n_in, void* d_out, int out_size,
                              void* d_ws, size_t ws_size, hipStream_t stream) {
    const u16* sv = (const u16*)d_in[0];
    const u16* pos = (const u16*)d_in[1];
    const int* src = (const int*)d_in[2];
    const int* dst = (const int*)d_in[3];
    const u16* freqs = (const u16*)d_in[4];
    const u16* w1 = (const u16*)d_in[5];
    const u16* w2 = (const u16*)d_in[6];
    const u16* wrbf = (const u16*)d_in[7];
    u16* out = (u16*)d_out;
    const int N = in_sizes[1] / 3;
    const int E = in_sizes[2];

#define CAPV 32
    int cap = CAPV;
    size_t off_cnt, off_wr, off_phi, off_slots, off_ent, off_om, off_oe, total;
    for (;;) {
        size_t o = 0;
        off_cnt = o; o = align_up(o + (size_t)(N + 1) * 4, 256);
        off_wr = o; o = align_up(o + 48 * 64 * 4, 256);
        off_phi = o; o = align_up(o + (size_t)N * 384 * 2, 256);
        off_slots = o; o = align_up(o + (size_t)N * cap * 8, 256);
        off_ent = o; o = align_up(o + (size_t)N * cap * 64, 256);
        off_om = o; o = align_up(o + (size_t)E * 16, 256);
        off_oe = o; o = align_up(o + (size_t)E * 64, 256);
        total = o;
        if (total <= ws_size || cap <= 16) break;
        cap >>= 1;
    }
    char* ws = (char*)d_ws;
    int* cnt = (int*)(ws + off_cnt);
    unsigned* wr = (unsigned*)(ws + off_wr);
    u16* phi = (u16*)(ws + off_phi);
    int2* slots = (int2*)(ws + off_slots);
    u32x4* entries = (u32x4*)(ws + off_ent);
    int4* ovf_meta = (int4*)(ws + off_om);
    u32x4* ovf_entries = (u32x4*)(ws + off_oe);

    const int tiles = (N + 31) / 32;
    const int front_blocks = (tiles + 3) / 4;
    (void)hipMemsetAsync(cnt, 0, align_up((size_t)(N + 1) * 4, 16), stream);
    const int fgrid = front_blocks < 256 ? 256 : front_blocks;
    k_front<<<fgrid, 512, 0, stream>>>(sv, w1, w2, wrbf, phi, wr, pos, src, dst, freqs, cnt, slots, entries, ovf_meta,
                                       ovf_entries, N, E, cap, front_blocks);
    int gblocks = (N + 3) / 4;
    if (gblocks > GBLOCKS) gblocks = GBLOCKS;
    k_gather<<<gblocks, 256, 0, stream>>>(sv, phi, cnt, slots, entries, (const u32x4*)wr, ovf_meta, ovf_entries, out,
                                          N, cap);
}
```
